```python
import torch

def _softmax(x, axis=-1):
    return torch.softmax(x, dim=axis)
def _gelu(x):
    return torch.nn.functional.gelu(x, approximate="tanh")
def _silu(x):
    return torch.nn.functional.silu(x)
def _sigmoid(x):
    return torch.sigmoid(x)
def _layernorm(x, axis=-1, eps=1e-5):
    m = x.mean(dim=axis, keepdim=True)
    v = x.var(dim=axis, keepdim=True, unbiased=False)
    return (x - m) / torch.sqrt(v + eps)
def _scatter_add(tgt, idx, src, axis=0):
    return tgt.clone().index_add_(axis, idx, src)
def _segment_sum(src, seg, n):
    out = torch.zeros((n,) + src.shape[1:], dtype=src.dtype)
    return out.index_add_(0, seg, src)
def _pad(x, pw):
    flat = tuple(v for pair in reversed(pw) for v in pair)
    return torch.nn.functional.pad(x, flat)

kernel_name = 'interleave_layers__spectral_fnet__spectral_fnet__prompt'

def setup_inputs():

    _w = (lambda a: (lambda t: t.to(torch.bfloat16) if t.is_floating_point() else t)(torch.as_tensor(a)))
    def _A_setup():
        B, S, D = 1, 2048, 640
        ns = torch.arange(S)[:, None] * torch.arange(S)[None, :]
        nd = torch.arange(D)[:, None] * torch.arange(D)[None, :]
        Ws_re = torch.cos(-2.0 * torch.pi * ns / S).to(float)
        Ws_im = torch.sin(-2.0 * torch.pi * ns / S).to(float)
        Wd_re = torch.cos(-2.0 * torch.pi * nd / D).to(float)
        Wd_im = torch.sin(-2.0 * torch.pi * nd / D).to(float)
        return {
            "x": _w((lambda *s: torch.randn(*s, dtype=torch.bfloat16) * 0.05)(B, S, D)),
            "dft_seq_re": _w(Ws_re),
            "dft_seq_im": _w(Ws_im),
            "dft_hid_re": _w(Wd_re),
            "dft_hid_im": _w(Wd_im),
        }
    _A_in = _A_setup()

    def _B_setup():
        B, S, D = 1, 2048, 640
        ns = torch.arange(S)[:, None] * torch.arange(S)[None, :]
        nd = torch.arange(D)[:, None] * torch.arange(D)[None, :]
        Ws_re = torch.cos(-2.0 * torch.pi * ns / S).to(float)
        Ws_im = torch.sin(-2.0 * torch.pi * ns / S).to(float)
        Wd_re = torch.cos(-2.0 * torch.pi * nd / D).to(float)
        Wd_im = torch.sin(-2.0 * torch.pi * nd / D).to(float)
        return {
            "x": _w((lambda *s: torch.randn(*s, dtype=torch.bfloat16) * 0.05)(B, S, D)),
            "dft_seq_re": _w(Ws_re),
            "dft_seq_im": _w(Ws_im),
            "dft_hid_re": _w(Wd_re),
            "dft_hid_im": _w(Wd_im),
        }
    _B_in = _B_setup()

    return {
        "A_x": _A_in["x"],
        "A_dft_seq_re": _A_in["dft_seq_re"],
        "A_dft_seq_im": _A_in["dft_seq_im"],
        "A_dft_hid_re": _A_in["dft_hid_re"],
        "A_dft_hid_im": _A_in["dft_hid_im"],
        "B_x": _B_in["x"],
        "B_dft_seq_re": _B_in["dft_seq_re"],
        "B_dft_seq_im": _B_in["dft_seq_im"],
        "B_dft_hid_re": _B_in["dft_hid_re"],
        "B_dft_hid_im": _B_in["dft_hid_im"],
    }

def reference(A_x, A_dft_seq_re, A_dft_seq_im, A_dft_hid_re, A_dft_hid_im, B_x, B_dft_seq_re, B_dft_seq_im, B_dft_hid_re, B_dft_hid_im):
    def _A_ref(x, dft_seq_re, dft_seq_im, dft_hid_re, dft_hid_im):
        a = torch.einsum("bsd,ed->bse", x, dft_hid_re)
        b = torch.einsum("bsd,ed->bse", x, dft_hid_im)
        y = torch.einsum("ts,bse->bte", dft_seq_re, a) - torch.einsum("ts,bse->bte", dft_seq_im, b)
        return y

    def _B_ref(x, dft_seq_re, dft_seq_im, dft_hid_re, dft_hid_im):
        a = torch.einsum("bsd,ed->bse", x, dft_hid_re)
        b = torch.einsum("bsd,ed->bse", x, dft_hid_im)
        y = torch.einsum("ts,bse->bte", dft_seq_re, a) - torch.einsum("ts,bse->bte", dft_seq_im, b)
        return y

    x = A_x
    x = x + _A_ref(_layernorm(x, axis=-1), A_dft_seq_re, A_dft_seq_im, A_dft_hid_re, A_dft_hid_im)
    x = x + _B_ref(_layernorm(x, axis=-1), B_dft_seq_re, B_dft_seq_im, B_dft_hid_re, B_dft_hid_im)
    x = x + _A_ref(_layernorm(x, axis=-1), A_dft_seq_re, A_dft_seq_im, A_dft_hid_re, A_dft_hid_im)
    x = x + _B_ref(_layernorm(x, axis=-1), B_dft_seq_re, B_dft_seq_im, B_dft_hid_re, B_dft_hid_im)
    return x
```

```cpp
#include <hip/hip_runtime.h>
#include <stdint.h>

#define LAS __attribute__((address_space(3)))

struct Args {
    const char* WreA; const char* WimA; const char* WreB; const char* WimB;
    char* ws; char* out; const char* x;
    const char* WdreA; const char* WdimA; const char* WdreB; const char* WdimB;
};

__global__ __launch_bounds__(256, 1) void fnet_kernel(Args a) {
    __shared__ __attribute__((aligned(1024))) char smem[159744];
    const int tid = threadIdx.x, w = __builtin_amdgcn_readfirstlane(tid >> 6), lane = tid & 63;
    const int bid = blockIdx.x, j = bid & 7, i = bid >> 3, g = 4 * j + (i >> 3), r = i & 7;
    const unsigned lbase = (unsigned)(uintptr_t)(LAS char*)smem;
    const unsigned long long ka = (unsigned long long)(uintptr_t)__builtin_amdgcn_kernarg_segment_ptr();
    (void)a;
    asm volatile(
"s_mov_b64 s[38:39], %0\n"
"v_mov_b32 v50, 0\n"
"s_mov_b32 s71, 0\n"
"s_load_dwordx8 s[20:27], s[38:39], 0\n"
"s_load_dwordx2 s[40:41], s[38:39], 32\n"
"s_load_dwordx2 s[52:53], s[38:39], 48\n"
"s_load_dwordx8 s[56:63], s[38:39], 56\n"
"s_mov_b32 s42, 36046848\n"
"s_mov_b32 s43, 0x00020000\n"
"s_mov_b32 s46, 8388608\n"
"s_mov_b32 s47, 0x00020000\n"
"s_mov_b32 s50, 8388608\n"
"s_mov_b32 s51, 0x00020000\n"
"s_mov_b32 s54, 2621440\n"
"s_mov_b32 s55, 0x00020000\n"
"s_mov_b32 s76, %1\n"
"s_mov_b32 s77, %2\n"
"s_mov_b32 s78, %3\n"
"s_mov_b32 s96, %4\n"
"s_mul_i32 s72, s78, 5120\n"
"s_add_u32 s37, s96, s72\n"
"s_add_u32 s37, s37, 98304\n"
"s_mul_i32 s72, s78, 24576\n"
"s_add_u32 s64, s96, s72\n"
"s_mov_b32 s84, 0\n"
"s_mov_b32 s85, 1\n"
"s_mov_b32 s71, 0\n"
"s_mov_b32 s86, 0xaaaaaaaa\n"
"s_mov_b32 s87, 0xaaaaaaaa\n"
"s_mov_b32 s88, 0xcccccccc\n"
"s_mov_b32 s89, 0xcccccccc\n"
"s_mov_b32 s90, 0xf0f0f0f0\n"
"s_mov_b32 s91, 0xf0f0f0f0\n"
"s_mov_b32 s92, 0xff00ff\n"
"s_mov_b32 s93, 0xff00ff\n"
"s_mov_b32 s98, 0x3c4ccccd\n"
"s_mov_b32 s99, 0x3c4ccccd\n"
"v_mov_b32 v244, %5\n"
"v_and_b32 v245, 15, v244\n"
"v_lshrrev_b32 v246, 4, v244\n"
"v_lshlrev_b32 v30, 4, v244\n"
"v_add_u32 v31, 4096, v30\n"
"s_lshl_b32 s72, s76, 6\n"
"s_lshl_b32 s73, s78, 4\n"
"s_add_u32 s72, s72, s73\n"
"v_add_u32 v247, 0, v246\n"
"v_xor_b32 v52, v245, v247\n"
"v_lshlrev_b32 v52, 4, v52\n"
"v_add_u32 v247, s72, v247\n"
"v_lshlrev_b32 v247, 12, v247\n"
"v_add_u32 v16, v52, v247\n"
"v_add_u32 v247, 4, v246\n"
"v_xor_b32 v52, v245, v247\n"
"v_lshlrev_b32 v52, 4, v52\n"
"v_add_u32 v247, s72, v247\n"
"v_lshlrev_b32 v247, 12, v247\n"
"v_add_u32 v17, v52, v247\n"
"v_subrev_u32 v17, 1024, v17\n"
"v_add_u32 v247, 8, v246\n"
"v_xor_b32 v52, v245, v247\n"
"v_lshlrev_b32 v52, 4, v52\n"
"v_add_u32 v247, s72, v247\n"
"v_lshlrev_b32 v247, 12, v247\n"
"v_add_u32 v18, v52, v247\n"
"v_subrev_u32 v18, 2048, v18\n"
"v_add_u32 v247, 12, v246\n"
"v_xor_b32 v52, v245, v247\n"
"v_lshlrev_b32 v52, 4, v52\n"
"v_add_u32 v247, s72, v247\n"
"v_lshlrev_b32 v247, 12, v247\n"
"v_add_u32 v19, v52, v247\n"
"v_subrev_u32 v19, 3072, v19\n"
"s_mul_i32 s72, s78, 5120\n"
"v_add_u32 v20, s72, v30\n"
"v_add_u32 v21, 4096, v20\n"
"v_min_u32 v22, 39, v244\n"
"s_and_b32 s72, s76, 3\n"
"s_mul_i32 s72, s72, 40\n"
"v_add_u32 v22, s72, v22\n"
"v_lshlrev_b32 v22, 7, v22\n"
"v_and_b32 v23, 31, v244\n"
"v_lshrrev_b32 v23, 1, v23\n"
"s_and_b32 s72, s77, 3\n"
"s_lshl_b32 s72, s72, 4\n"
"s_lshl_b32 s73, s76, 6\n"
"s_add_u32 s72, s72, s73\n"
"v_add_u32 v23, s72, v23\n"
"v_lshlrev_b32 v23, 12, v23\n"
"v_and_b32 v52, 1, v244\n"
"v_lshlrev_b32 v52, 7, v52\n"
"v_add_u32 v23, v23, v52\n"
"v_lshlrev_b32 v247, 8, v245\n"
"v_add_u32 v247, s64, v247\n"
"v_add_u32 v52, 0, v246\n"
"v_xor_b32 v52, v52, v245\n"
"v_lshlrev_b32 v52, 4, v52\n"
"v_add_u32 v25, v52, v247\n"
"v_add_u32 v52, 4, v246\n"
"v_xor_b32 v52, v52, v245\n"
"v_lshlrev_b32 v52, 4, v52\n"
"v_add_u32 v26, v52, v247\n"
"v_add_u32 v52, 8, v246\n"
"v_xor_b32 v52, v52, v245\n"
"v_lshlrev_b32 v52, 4, v52\n"
"v_add_u32 v27, v52, v247\n"
"v_add_u32 v52, 12, v246\n"
"v_xor_b32 v52, v52, v245\n"
"v_lshlrev_b32 v52, 4, v52\n"
"v_add_u32 v28, v52, v247\n"
"v_mul_u32_u24 v52, 1280, v246\n"
"v_lshlrev_b32 v247, 4, v245\n"
"v_add_u32 v52, v52, v247\n"
"v_add_u32 v52, s96, v52\n"
"v_add_u32 v29, 98304, v52\n"
"v_add_u32 v51, 40960, v29\n"
"v_lshrrev_b32 v52, 1, v246\n"
"s_lshl_b32 s72, s78, 1\n"
"v_add_u32 v52, s72, v52\n"
"v_mul_u32_u24 v52, 1280, v52\n"
"v_add_u32 v52, v52, v247\n"
"v_and_b32 v53, 1, v246\n"
"v_lshlrev_b32 v54, 3, v53\n"
"v_add_u32 v42, v52, v54\n"
"v_lshlrev_b32 v54, 8, v53\n"
"v_add_u32 v43, v52, v54\n"
"v_and_b32 v52, 3, v245\n"
"v_lshlrev_b32 v53, 2, v246\n"
"v_add_u32 v52, v52, v53\n"
"s_lshl_b32 s72, s78, 4\n"
"v_add_u32 v52, s72, v52\n"
"v_lshlrev_b32 v44, 4, v52\n"
"v_lshrrev_b32 v52, 2, v245\n"
"v_and_b32 v52, 1, v52\n"
"v_lshlrev_b32 v52, 3, v52\n"
"v_add_u32 v44, v44, v52\n"
"v_add_u32 v52, s72, v245\n"
"v_lshlrev_b32 v52, 4, v52\n"
"v_lshlrev_b32 v45, 11, v246\n"
"v_add_u32 v45, v45, v52\n"
"v_lshlrev_b32 v46, 4, v246\n"
"s_mul_i32 s72, s78, 15360\n"
"s_add_u32 s72, s72, s96\n"
"s_add_u32 s72, s72, 98304\n"
"v_add_u32 v47, s72, v30\n"
"v_and_b32 v48, 7, v244\n"
"v_mul_u32_u24 v48, 4, v48\n"
"v_and_b32 v49, 31, v244\n"
"v_mul_u32_u24 v49, 4, v49\n"
"s_waitcnt lgkmcnt(0)\n"
"v_mov_b32 v52, 0\n"
"s_mov_b32 s72, 35783680\n"
"buffer_load_dword v56, v52, s[40:43], s72 offen sc1\n"
"v_lshrrev_b32 v52, 2, v244\n"
"s_lshl_b32 s72, s78, 4\n"
"v_add_u32 v52, s72, v52\n"
"v_mul_u32_u24 v52, 1280, v52\n"
"v_and_b32 v53, 3, v244\n"
"v_lshlrev_b32 v53, 4, v53\n"
"v_add_u32 v52, v52, v53\n"
"s_mul_i32 s73, s76, 81920\n"
"buffer_load_dwordx4 v[64:67], v52, s[52:55], s73 offen\n"
"buffer_load_dwordx4 v[68:71], v52, s[52:55], s73 offen offset:64\n"
"buffer_load_dwordx4 v[72:75], v52, s[52:55], s73 offen offset:128\n"
"buffer_load_dwordx4 v[76:79], v52, s[52:55], s73 offen offset:192\n"
"buffer_load_dwordx4 v[80:83], v52, s[52:55], s73 offen offset:256\n"
"buffer_load_dwordx4 v[84:87], v52, s[52:55], s73 offen offset:320\n"
"buffer_load_dwordx4 v[88:91], v52, s[52:55], s73 offen offset:384\n"
"buffer_load_dwordx4 v[92:95], v52, s[52:55], s73 offen offset:448\n"
"buffer_load_dwordx4 v[96:99], v52, s[52:55], s73 offen offset:512\n"
"buffer_load_dwordx4 v[100:103], v52, s[52:55], s73 offen offset:576\n"
"buffer_load_dwordx4 v[104:107], v52, s[52:55], s73 offen offset:640\n"
"buffer_load_dwordx4 v[108:111], v52, s[52:55], s73 offen offset:704\n"
"buffer_load_dwordx4 v[112:115], v52, s[52:55], s73 offen offset:768\n"
"buffer_load_dwordx4 v[116:119], v52, s[52:55], s73 offen offset:832\n"
"buffer_load_dwordx4 v[120:123], v52, s[52:55], s73 offen offset:896\n"
"buffer_load_dwordx4 v[124:127], v52, s[52:55], s73 offen offset:960\n"
"buffer_load_dwordx4 v[128:131], v52, s[52:55], s73 offen offset:1024\n"
"buffer_load_dwordx4 v[132:135], v52, s[52:55], s73 offen offset:1088\n"
"buffer_load_dwordx4 v[136:139], v52, s[52:55], s73 offen offset:1152\n"
"buffer_load_dwordx4 v[140:143], v52, s[52:55], s73 offen offset:1216\n"
"s_mul_i32 s74, s77, 160\n"
"s_add_u32 s74, s74, s73\n"
"s_mul_i32 s72, s78, 20480\n"
"s_add_u32 s74, s74, s72\n"
"s_mul_i32 s72, s78, 3072\n"
"s_add_u32 s72, s72, s96\n"
"s_add_u32 s72, s72, 98304\n"
"v_add_u32 v53, 0, v244\n"
"v_min_u32 v53, 159, v53\n"
"v_mul_u32_u24 v54, 6554, v53\n"
"v_lshrrev_b32 v54, 16, v54\n"
"v_mul_u32_u24 v55, 10, v54\n"
"v_sub_u32 v55, v53, v55\n"
"v_lshlrev_b32 v55, 4, v55\n"
"v_mul_u32_u24 v54, 1280, v54\n"
"v_add_u32 v54, v54, v55\n"
"s_add_u32 m0, s72, 0\n"
"s_nop 0\n"
"buffer_load_dwordx4 v54, s[52:55], s74 offen lds\n"
"v_add_u32 v53, 64, v244\n"
"v_min_u32 v53, 159, v53\n"
"v_mul_u32_u24 v54, 6554, v53\n"
"v_lshrrev_b32 v54, 16, v54\n"
"v_mul_u32_u24 v55, 10, v54\n"
"v_sub_u32 v55, v53, v55\n"
"v_lshlrev_b32 v55, 4, v55\n"
"v_mul_u32_u24 v54, 1280, v54\n"
"v_add_u32 v54, v54, v55\n"
"s_add_u32 m0, s72, 1024\n"
"s_nop 0\n"
"buffer_load_dwordx4 v54, s[52:55], s74 offen lds\n"
"v_add_u32 v53, 128, v244\n"
"v_min_u32 v53, 159, v53\n"
"v_mul_u32_u24 v54, 6554, v53\n"
"v_lshrrev_b32 v54, 16, v54\n"
"v_mul_u32_u24 v55, 10, v54\n"
"v_sub_u32 v55, v53, v55\n"
"v_lshlrev_b32 v55, 4, v55\n"
"v_mul_u32_u24 v54, 1280, v54\n"
"v_add_u32 v54, v54, v55\n"
"s_add_u32 m0, s72, 2048\n"
"s_nop 0\n"
"buffer_load_dwordx4 v54, s[52:55], s74 offen lds\n"
"s_mov_b32 s82, 819200\n"
"s_mov_b32 s83, 0x00020000\n"
"v_lshrrev_b32 v54, 2, v244\n"
"v_mul_u32_u24 v54, 1280, v54\n"
"v_and_b32 v55, 3, v244\n"
"v_lshlrev_b32 v55, 3, v55\n"
"v_add_u32 v54, v54, v55\n"
"s_add_u32 s66, s78, 0\n"
"s_lshl_b32 s66, s66, 5\n"
"s_add_u32 s66, s66, s76\n"
"s_min_u32 s66, s66, 399\n"
"s_cmp_ge_u32 s66, 200\n"
"s_cselect_b32 s67, 1, 0\n"
"s_mul_i32 s79, s67, 200\n"
"s_sub_u32 s30, s66, s79\n"
"s_mul_i32 s79, s30, 5243\n"
"s_lshr_b32 s79, s79, 17\n"
"s_mul_i32 s28, s79, 25\n"
"s_sub_u32 s28, s30, s28\n"
"s_mul_i32 s30, s28, 52\n"
"s_lshr_b32 s30, s30, 8\n"
"s_mul_i32 s29, s30, 5\n"
"s_sub_u32 s28, s28, s29\n"
"s_lshl_b32 s70, s30, 3\n"
"s_cmp_ge_u32 s70, 20\n"
"s_cselect_b32 s29, 1, 0\n"
"s_mul_i32 s72, s29, 20\n"
"s_sub_u32 s70, s70, s72\n"
"s_lshr_b32 s70, s70, 2\n"
"s_cmp_eq_u32 s29, 0\n"
"s_cselect_b64 s[80:81], s[56:57], s[58:59]\n"
"s_cselect_b64 s[94:95], s[60:61], s[62:63]\n"
"s_cmp_eq_u32 s67, 0\n"
"s_cselect_b64 s[80:81], s[80:81], s[94:95]\n"
"s_mul_i32 s72, s77, 80\n"
"s_lshl_b32 s73, s28, 4\n"
"s_add_u32 s72, s72, s73\n"
"s_mul_i32 s72, s72, 1280\n"
"s_mul_i32 s73, s79, 160\n"
"s_add_u32 s72, s72, s73\n"
"s_lshl_b32 s73, s70, 5\n"
"s_add_u32 s72, s72, s73\n"
"buffer_load_dwordx2 v[164:165], v54, s[80:83], s72 offen\n"
"s_cmp_eq_u32 s29, 1\n"
"s_cselect_b32 s73, 0x80008000, 0\n"
"v_mov_b32 v180, s73\n"
"s_lshl_b32 s70, s30, 3\n"
"s_add_u32 s70, s70, 4\n"
"s_cmp_ge_u32 s70, 20\n"
"s_cselect_b32 s29, 1, 0\n"
"s_mul_i32 s72, s29, 20\n"
"s_sub_u32 s70, s70, s72\n"
"s_lshr_b32 s70, s70, 2\n"
"s_cmp_eq_u32 s29, 0\n"
"s_cselect_b64 s[80:81], s[56:57], s[58:59]\n"
"s_cselect_b64 s[94:95], s[60:61], s[62:63]\n"
"s_cmp_eq_u32 s67, 0\n"
"s_cselect_b64 s[80:81], s[80:81], s[94:95]\n"
"s_mul_i32 s72, s77, 80\n"
"s_lshl_b32 s73, s28, 4\n"
"s_add_u32 s72, s72, s73\n"
"s_mul_i32 s72, s72, 1280\n"
"s_mul_i32 s73, s79, 160\n"
"s_add_u32 s72, s72, s73\n"
"s_lshl_b32 s73, s70, 5\n"
"s_add_u32 s72, s72, s73\n"
"buffer_load_dwordx2 v[166:167], v54, s[80:83], s72 offen\n"
"s_cmp_eq_u32 s29, 1\n"
"s_cselect_b32 s73, 0x80008000, 0\n"
"v_mov_b32 v181, s73\n"
"s_lshl_b32 s72, s77, 3\n"
"s_add_u32 s72, s72, s79\n"
"s_mul_i32 s72, s72, 25\n"
"s_mul_i32 s73, s30, 5\n"
"s_add_u32 s72, s72, s73\n"
"s_add_u32 s72, s72, s28\n"
"s_lshl_b32 s72, s72, 10\n"
"s_mul_i32 s73, s67, 1638400\n"
"s_add_u32 s72, s72, s73\n"
"s_add_u32 s72, s72, 31457280\n"
"v_mov_b32 v188, s72\n"
"s_add_u32 s66, s78, 4\n"
"s_lshl_b32 s66, s66, 5\n"
"s_add_u32 s66, s66, s76\n"
"s_min_u32 s66, s66, 399\n"
"s_cmp_ge_u32 s66, 200\n"
"s_cselect_b32 s67, 1, 0\n"
"s_mul_i32 s79, s67, 200\n"
"s_sub_u32 s30, s66, s79\n"
"s_mul_i32 s79, s30, 5243\n"
"s_lshr_b32 s79, s79, 17\n"
"s_mul_i32 s28, s79, 25\n"
"s_sub_u32 s28, s30, s28\n"
"s_mul_i32 s30, s28, 52\n"
"s_lshr_b32 s30, s30, 8\n"
"s_mul_i32 s29, s30, 5\n"
"s_sub_u32 s28, s28, s29\n"
"s_lshl_b32 s70, s30, 3\n"
"s_cmp_ge_u32 s70, 20\n"
"s_cselect_b32 s29, 1, 0\n"
"s_mul_i32 s72, s29, 20\n"
"s_sub_u32 s70, s70, s72\n"
"s_lshr_b32 s70, s70, 2\n"
"s_cmp_eq_u32 s29, 0\n"
"s_cselect_b64 s[80:81], s[56:57], s[58:59]\n"
"s_cselect_b64 s[94:95], s[60:61], s[62:63]\n"
"s_cmp_eq_u32 s67, 0\n"
"s_cselect_b64 s[80:81], s[80:81], s[94:95]\n"
"s_mul_i32 s72, s77, 80\n"
"s_lshl_b32 s73, s28, 4\n"
"s_add_u32 s72, s72, s73\n"
"s_mul_i32 s72, s72, 1280\n"
"s_mul_i32 s73, s79, 160\n"
"s_add_u32 s72, s72, s73\n"
"s_lshl_b32 s73, s70, 5\n"
"s_add_u32 s72, s72, s73\n"
"buffer_load_dwordx2 v[168:169], v54, s[80:83], s72 offen\n"
"s_cmp_eq_u32 s29, 1\n"
"s_cselect_b32 s73, 0x80008000, 0\n"
"v_mov_b32 v182, s73\n"
"s_lshl_b32 s70, s30, 3\n"
"s_add_u32 s70, s70, 4\n"
"s_cmp_ge_u32 s70, 20\n"
"s_cselect_b32 s29, 1, 0\n"
"s_mul_i32 s72, s29, 20\n"
"s_sub_u32 s70, s70, s72\n"
"s_lshr_b32 s70, s70, 2\n"
"s_cmp_eq_u32 s29, 0\n"
"s_cselect_b64 s[80:81], s[56:57], s[58:59]\n"
"s_cselect_b64 s[94:95], s[60:61], s[62:63]\n"
"s_cmp_eq_u32 s67, 0\n"
"s_cselect_b64 s[80:81], s[80:81], s[94:95]\n"
"s_mul_i32 s72, s77, 80\n"
"s_lshl_b32 s73, s28, 4\n"
"s_add_u32 s72, s72, s73\n"
"s_mul_i32 s72, s72, 1280\n"
"s_mul_i32 s73, s79, 160\n"
"s_add_u32 s72, s72, s73\n"
"s_lshl_b32 s73, s70, 5\n"
"s_add_u32 s72, s72, s73\n"
"buffer_load_dwordx2 v[170:171], v54, s[80:83], s72 offen\n"
"s_cmp_eq_u32 s29, 1\n"
"s_cselect_b32 s73, 0x80008000, 0\n"
"v_mov_b32 v183, s73\n"
"s_lshl_b32 s72, s77, 3\n"
"s_add_u32 s72, s72, s79\n"
"s_mul_i32 s72, s72, 25\n"
"s_mul_i32 s73, s30, 5\n"
"s_add_u32 s72, s72, s73\n"
"s_add_u32 s72, s72, s28\n"
"s_lshl_b32 s72, s72, 10\n"
"s_mul_i32 s73, s67, 1638400\n"
"s_add_u32 s72, s72, s73\n"
"s_add_u32 s72, s72, 31457280\n"
"v_mov_b32 v189, s72\n"
"s_add_u32 s66, s78, 8\n"
"s_lshl_b32 s66, s66, 5\n"
"s_add_u32 s66, s66, s76\n"
"s_min_u32 s66, s66, 399\n"
"s_cmp_ge_u32 s66, 200\n"
"s_cselect_b32 s67, 1, 0\n"
"s_mul_i32 s79, s67, 200\n"
"s_sub_u32 s30, s66, s79\n"
"s_mul_i32 s79, s30, 5243\n"
"s_lshr_b32 s79, s79, 17\n"
"s_mul_i32 s28, s79, 25\n"
"s_sub_u32 s28, s30, s28\n"
"s_mul_i32 s30, s28, 52\n"
"s_lshr_b32 s30, s30, 8\n"
"s_mul_i32 s29, s30, 5\n"
"s_sub_u32 s28, s28, s29\n"
"s_lshl_b32 s70, s30, 3\n"
"s_cmp_ge_u32 s70, 20\n"
"s_cselect_b32 s29, 1, 0\n"
"s_mul_i32 s72, s29, 20\n"
"s_sub_u32 s70, s70, s72\n"
"s_lshr_b32 s70, s70, 2\n"
"s_cmp_eq_u32 s29, 0\n"
"s_cselect_b64 s[80:81], s[56:57], s[58:59]\n"
"s_cselect_b64 s[94:95], s[60:61], s[62:63]\n"
"s_cmp_eq_u32 s67, 0\n"
"s_cselect_b64 s[80:81], s[80:81], s[94:95]\n"
"s_mul_i32 s72, s77, 80\n"
"s_lshl_b32 s73, s28, 4\n"
"s_add_u32 s72, s72, s73\n"
"s_mul_i32 s72, s72, 1280\n"
"s_mul_i32 s73, s79, 160\n"
"s_add_u32 s72, s72, s73\n"
"s_lshl_b32 s73, s70, 5\n"
"s_add_u32 s72, s72, s73\n"
"buffer_load_dwordx2 v[172:173], v54, s[80:83], s72 offen\n"
"s_cmp_eq_u32 s29, 1\n"
"s_cselect_b32 s73, 0x80008000, 0\n"
"v_mov_b32 v184, s73\n"
"s_lshl_b32 s70, s30, 3\n"
"s_add_u32 s70, s70, 4\n"
"s_cmp_ge_u32 s70, 20\n"
"s_cselect_b32 s29, 1, 0\n"
"s_mul_i32 s72, s29, 20\n"
"s_sub_u32 s70, s70, s72\n"
"s_lshr_b32 s70, s70, 2\n"
"s_cmp_eq_u32 s29, 0\n"
"s_cselect_b64 s[80:81], s[56:57], s[58:59]\n"
"s_cselect_b64 s[94:95], s[60:61], s[62:63]\n"
"s_cmp_eq_u32 s67, 0\n"
"s_cselect_b64 s[80:81], s[80:81], s[94:95]\n"
"s_mul_i32 s72, s77, 80\n"
"s_lshl_b32 s73, s28, 4\n"
"s_add_u32 s72, s72, s73\n"
"s_mul_i32 s72, s72, 1280\n"
"s_mul_i32 s73, s79, 160\n"
"s_add_u32 s72, s72, s73\n"
"s_lshl_b32 s73, s70, 5\n"
"s_add_u32 s72, s72, s73\n"
"buffer_load_dwordx2 v[174:175], v54, s[80:83], s72 offen\n"
"s_cmp_eq_u32 s29, 1\n"
"s_cselect_b32 s73, 0x80008000, 0\n"
"v_mov_b32 v185, s73\n"
"s_lshl_b32 s72, s77, 3\n"
"s_add_u32 s72, s72, s79\n"
"s_mul_i32 s72, s72, 25\n"
"s_mul_i32 s73, s30, 5\n"
"s_add_u32 s72, s72, s73\n"
"s_add_u32 s72, s72, s28\n"
"s_lshl_b32 s72, s72, 10\n"
"s_mul_i32 s73, s67, 1638400\n"
"s_add_u32 s72, s72, s73\n"
"s_add_u32 s72, s72, 31457280\n"
"v_mov_b32 v190, s72\n"
"s_add_u32 s66, s78, 12\n"
"s_lshl_b32 s66, s66, 5\n"
"s_add_u32 s66, s66, s76\n"
"s_min_u32 s66, s66, 399\n"
"s_cmp_ge_u32 s66, 200\n"
"s_cselect_b32 s67, 1, 0\n"
"s_mul_i32 s79, s67, 200\n"
"s_sub_u32 s30, s66, s79\n"
"s_mul_i32 s79, s30, 5243\n"
"s_lshr_b32 s79, s79, 17\n"
"s_mul_i32 s28, s79, 25\n"
"s_sub_u32 s28, s30, s28\n"
"s_mul_i32 s30, s28, 52\n"
"s_lshr_b32 s30, s30, 8\n"
"s_mul_i32 s29, s30, 5\n"
"s_sub_u32 s28, s28, s29\n"
"s_lshl_b32 s70, s30, 3\n"
"s_cmp_ge_u32 s70, 20\n"
"s_cselect_b32 s29, 1, 0\n"
"s_mul_i32 s72, s29, 20\n"
"s_sub_u32 s70, s70, s72\n"
"s_lshr_b32 s70, s70, 2\n"
"s_cmp_eq_u32 s29, 0\n"
"s_cselect_b64 s[80:81], s[56:57], s[58:59]\n"
"s_cselect_b64 s[94:95], s[60:61], s[62:63]\n"
"s_cmp_eq_u32 s67, 0\n"
"s_cselect_b64 s[80:81], s[80:81], s[94:95]\n"
"s_mul_i32 s72, s77, 80\n"
"s_lshl_b32 s73, s28, 4\n"
"s_add_u32 s72, s72, s73\n"
"s_mul_i32 s72, s72, 1280\n"
"s_mul_i32 s73, s79, 160\n"
"s_add_u32 s72, s72, s73\n"
"s_lshl_b32 s73, s70, 5\n"
"s_add_u32 s72, s72, s73\n"
"buffer_load_dwordx2 v[176:177], v54, s[80:83], s72 offen\n"
"s_cmp_eq_u32 s29, 1\n"
"s_cselect_b32 s73, 0x80008000, 0\n"
"v_mov_b32 v186, s73\n"
"s_lshl_b32 s70, s30, 3\n"
"s_add_u32 s70, s70, 4\n"
"s_cmp_ge_u32 s70, 20\n"
"s_cselect_b32 s29, 1, 0\n"
"s_mul_i32 s72, s29, 20\n"
"s_sub_u32 s70, s70, s72\n"
"s_lshr_b32 s70, s70, 2\n"
"s_cmp_eq_u32 s29, 0\n"
"s_cselect_b64 s[80:81], s[56:57], s[58:59]\n"
"s_cselect_b64 s[94:95], s[60:61], s[62:63]\n"
"s_cmp_eq_u32 s67, 0\n"
"s_cselect_b64 s[80:81], s[80:81], s[94:95]\n"
"s_mul_i32 s72, s77, 80\n"
"s_lshl_b32 s73, s28, 4\n"
"s_add_u32 s72, s72, s73\n"
"s_mul_i32 s72, s72, 1280\n"
"s_mul_i32 s73, s79, 160\n"
"s_add_u32 s72, s72, s73\n"
"s_lshl_b32 s73, s70, 5\n"
"s_add_u32 s72, s72, s73\n"
"buffer_load_dwordx2 v[178:179], v54, s[80:83], s72 offen\n"
"s_cmp_eq_u32 s29, 1\n"
"s_cselect_b32 s73, 0x80008000, 0\n"
"v_mov_b32 v187, s73\n"
"s_lshl_b32 s72, s77, 3\n"
"s_add_u32 s72, s72, s79\n"
"s_mul_i32 s72, s72, 25\n"
"s_mul_i32 s73, s30, 5\n"
"s_add_u32 s72, s72, s73\n"
"s_add_u32 s72, s72, s28\n"
"s_lshl_b32 s72, s72, 10\n"
"s_mul_i32 s73, s67, 1638400\n"
"s_add_u32 s72, s72, s73\n"
"s_add_u32 s72, s72, 31457280\n"
"v_mov_b32 v191, s72\n"
"s_and_b32 s72, s71, 1\n"
"s_cmp_eq_u32 s72, 0\n"
"s_cselect_b64 s[44:45], s[20:21], s[24:25]\n"
"s_cselect_b64 s[48:49], s[22:23], s[26:27]\n"
"s_cmp_lt_u32 s71, 2\n"
"s_cselect_b32 s75, 2, 0\n"
"s_mul_i32 s75, s75, s77\n"
"s_lshl_b32 s75, s75, 8\n"
"s_and_b32 s75, s75, 0xfff\n"
"s_mov_b32 s65, s75\n"
"v_mov_b32 v192, 0\n"
"v_mov_b32 v193, 0\n"
"v_mov_b32 v194, 0\n"
"v_mov_b32 v195, 0\n"
"s_lshl_b32 s72, s76, 3\n"
"s_add_u32 s72, s72, s77\n"
"s_lshl_b32 s72, s72, 12\n"
"s_lshl_b32 s73, s78, 10\n"
"s_add_u32 s72, s72, s73\n"
"s_add_u32 s66, s72, 34734080\n"
"v_mov_b32 v196, 0x3f803f80\n"
"v_mov_b32 v200, 0\n"
"v_mov_b32 v204, 0\n"
"v_mov_b32 v201, 0\n"
"v_mov_b32 v205, 0\n"
"v_mov_b32 v202, 0\n"
"v_mov_b32 v206, 0\n"
"v_mov_b32 v203, 0\n"
"v_mov_b32 v207, 0\n"
"s_waitcnt vmcnt(11)\n"
"v_readfirstlane_b32 s36, v56\n"
"s_nop 4\n"
"s_add_u32 s36, s36, 1\n"
"v_dot2_f32_bf16 v200, v64, v196, v200\n"
"v_dot2_f32_bf16 v204, v64, v64, v204\n"
"v_dot2_f32_bf16 v201, v65, v196, v201\n"
"buffer_store_dwordx4 v[192:195], v30, s[40:43], s66 offen sc1\n"
"v_dot2_f32_bf16 v205, v65, v65, v205\n"
"v_dot2_f32_bf16 v202, v66, v196, v202\n"
"v_dot2_f32_bf16 v206, v66, v66, v206\n"
"v_dot2_f32_bf16 v203, v67, v196, v203\n"
"v_dot2_f32_bf16 v207, v67, v67, v207\n"
"v_dot2_f32_bf16 v200, v68, v196, v200\n"
"v_dot2_f32_bf16 v204, v68, v68, v204\n"
"v_dot2_f32_bf16 v201, v69, v196, v201\n"
"v_dot2_f32_bf16 v205, v69, v69, v205\n"
"v_dot2_f32_bf16 v202, v70, v196, v202\n"
"v_dot2_f32_bf16 v206, v70, v70, v206\n"
"v_dot2_f32_bf16 v203, v71, v196, v203\n"
"v_dot2_f32_bf16 v207, v71, v71, v207\n"
"v_dot2_f32_bf16 v200, v72, v196, v200\n"
"v_dot2_f32_bf16 v204, v72, v72, v204\n"
"v_dot2_f32_bf16 v201, v73, v196, v201\n"
"v_dot2_f32_bf16 v205, v73, v73, v205\n"
"v_dot2_f32_bf16 v202, v74, v196, v202\n"
"v_dot2_f32_bf16 v206, v74, v74, v206\n"
"v_dot2_f32_bf16 v203, v75, v196, v203\n"
"v_dot2_f32_bf16 v207, v75, v75, v207\n"
"v_dot2_f32_bf16 v200, v76, v196, v200\n"
"v_dot2_f32_bf16 v204, v76, v76, v204\n"
"v_dot2_f32_bf16 v201, v77, v196, v201\n"
"v_dot2_f32_bf16 v205, v77, v77, v205\n"
"v_dot2_f32_bf16 v202, v78, v196, v202\n"
"v_dot2_f32_bf16 v206, v78, v78, v206\n"
"v_dot2_f32_bf16 v203, v79, v196, v203\n"
"v_dot2_f32_bf16 v207, v79, v79, v207\n"
"v_dot2_f32_bf16 v200, v80, v196, v200\n"
"v_dot2_f32_bf16 v204, v80, v80, v204\n"
"v_dot2_f32_bf16 v201, v81, v196, v201\n"
"v_dot2_f32_bf16 v205, v81, v81, v205\n"
"v_dot2_f32_bf16 v202, v82, v196, v202\n"
"v_dot2_f32_bf16 v206, v82, v82, v206\n"
"v_dot2_f32_bf16 v203, v83, v196, v203\n"
"v_dot2_f32_bf16 v207, v83, v83, v207\n"
"v_dot2_f32_bf16 v200, v84, v196, v200\n"
"v_dot2_f32_bf16 v204, v84, v84, v204\n"
"v_dot2_f32_bf16 v201, v85, v196, v201\n"
"v_dot2_f32_bf16 v205, v85, v85, v205\n"
"v_dot2_f32_bf16 v202, v86, v196, v202\n"
"v_dot2_f32_bf16 v206, v86, v86, v206\n"
"v_dot2_f32_bf16 v203, v87, v196, v203\n"
"v_dot2_f32_bf16 v207, v87, v87, v207\n"
"v_dot2_f32_bf16 v200, v88, v196, v200\n"
"v_dot2_f32_bf16 v204, v88, v88, v204\n"
"v_dot2_f32_bf16 v201, v89, v196, v201\n"
"v_dot2_f32_bf16 v205, v89, v89, v205\n"
"v_dot2_f32_bf16 v202, v90, v196, v202\n"
"v_dot2_f32_bf16 v206, v90, v90, v206\n"
"v_dot2_f32_bf16 v203, v91, v196, v203\n"
"v_dot2_f32_bf16 v207, v91, v91, v207\n"
"v_dot2_f32_bf16 v200, v92, v196, v200\n"
"v_dot2_f32_bf16 v204, v92, v92, v204\n"
"v_dot2_f32_bf16 v201, v93, v196, v201\n"
"v_dot2_f32_bf16 v205, v93, v93, v205\n"
"v_dot2_f32_bf16 v202, v94, v196, v202\n"
"v_dot2_f32_bf16 v206, v94, v94, v206\n"
"v_dot2_f32_bf16 v203, v95, v196, v203\n"
"v_dot2_f32_bf16 v207, v95, v95, v207\n"
"v_dot2_f32_bf16 v200, v96, v196, v200\n"
"v_dot2_f32_bf16 v204, v96, v96, v204\n"
"v_dot2_f32_bf16 v201, v97, v196, v201\n"
"v_dot2_f32_bf16 v205, v97, v97, v205\n"
"v_dot2_f32_bf16 v202, v98, v196, v202\n"
"v_dot2_f32_bf16 v206, v98, v98, v206\n"
"v_dot2_f32_bf16 v203, v99, v196, v203\n"
"v_dot2_f32_bf16 v207, v99, v99, v207\n"
"v_dot2_f32_bf16 v200, v100, v196, v200\n"
"v_dot2_f32_bf16 v204, v100, v100, v204\n"
"v_dot2_f32_bf16 v201, v101, v196, v201\n"
"v_dot2_f32_bf16 v205, v101, v101, v205\n"
"v_dot2_f32_bf16 v202, v102, v196, v202\n"
"v_dot2_f32_bf16 v206, v102, v102, v206\n"
"v_dot2_f32_bf16 v203, v103, v196, v203\n"
"v_dot2_f32_bf16 v207, v103, v103, v207\n"
"v_dot2_f32_bf16 v200, v104, v196, v200\n"
"v_dot2_f32_bf16 v204, v104, v104, v204\n"
"v_dot2_f32_bf16 v201, v105, v196, v201\n"
"v_dot2_f32_bf16 v205, v105, v105, v205\n"
"v_dot2_f32_bf16 v202, v106, v196, v202\n"
"v_dot2_f32_bf16 v206, v106, v106, v206\n"
"v_dot2_f32_bf16 v203, v107, v196, v203\n"
"v_dot2_f32_bf16 v207, v107, v107, v207\n"
"v_dot2_f32_bf16 v200, v108, v196, v200\n"
"v_dot2_f32_bf16 v204, v108, v108, v204\n"
"v_dot2_f32_bf16 v201, v109, v196, v201\n"
"v_dot2_f32_bf16 v205, v109, v109, v205\n"
"v_dot2_f32_bf16 v202, v110, v196, v202\n"
"v_dot2_f32_bf16 v206, v110, v110, v206\n"
"v_dot2_f32_bf16 v203, v111, v196, v203\n"
"v_dot2_f32_bf16 v207, v111, v111, v207\n"
"v_dot2_f32_bf16 v200, v112, v196, v200\n"
"v_dot2_f32_bf16 v204, v112, v112, v204\n"
"v_dot2_f32_bf16 v201, v113, v196, v201\n"
"v_dot2_f32_bf16 v205, v113, v113, v205\n"
"v_dot2_f32_bf16 v202, v114, v196, v202\n"
"v_dot2_f32_bf16 v206, v114, v114, v206\n"
"v_dot2_f32_bf16 v203, v115, v196, v203\n"
"v_dot2_f32_bf16 v207, v115, v115, v207\n"
"v_dot2_f32_bf16 v200, v116, v196, v200\n"
"v_dot2_f32_bf16 v204, v116, v116, v204\n"
"v_dot2_f32_bf16 v201, v117, v196, v201\n"
"v_dot2_f32_bf16 v205, v117, v117, v205\n"
"v_dot2_f32_bf16 v202, v118, v196, v202\n"
"v_dot2_f32_bf16 v206, v118, v118, v206\n"
"v_dot2_f32_bf16 v203, v119, v196, v203\n"
"v_dot2_f32_bf16 v207, v119, v119, v207\n"
"v_dot2_f32_bf16 v200, v120, v196, v200\n"
"v_dot2_f32_bf16 v204, v120, v120, v204\n"
"v_dot2_f32_bf16 v201, v121, v196, v201\n"
"v_dot2_f32_bf16 v205, v121, v121, v205\n"
"v_dot2_f32_bf16 v202, v122, v196, v202\n"
"v_dot2_f32_bf16 v206, v122, v122, v206\n"
"v_dot2_f32_bf16 v203, v123, v196, v203\n"
"v_dot2_f32_bf16 v207, v123, v123, v207\n"
"v_dot2_f32_bf16 v200, v124, v196, v200\n"
"v_dot2_f32_bf16 v204, v124, v124, v204\n"
"v_dot2_f32_bf16 v201, v125, v196, v201\n"
"v_dot2_f32_bf16 v205, v125, v125, v205\n"
"v_dot2_f32_bf16 v202, v126, v196, v202\n"
"v_dot2_f32_bf16 v206, v126, v126, v206\n"
"v_dot2_f32_bf16 v203, v127, v196, v203\n"
"v_dot2_f32_bf16 v207, v127, v127, v207\n"
"v_dot2_f32_bf16 v200, v128, v196, v200\n"
"v_dot2_f32_bf16 v204, v128, v128, v204\n"
"v_dot2_f32_bf16 v201, v129, v196, v201\n"
"v_dot2_f32_bf16 v205, v129, v129, v205\n"
"v_dot2_f32_bf16 v202, v130, v196, v202\n"
"v_dot2_f32_bf16 v206, v130, v130, v206\n"
"v_dot2_f32_bf16 v203, v131, v196, v203\n"
"v_dot2_f32_bf16 v207, v131, v131, v207\n"
"v_dot2_f32_bf16 v200, v132, v196, v200\n"
"v_dot2_f32_bf16 v204, v132, v132, v204\n"
"v_dot2_f32_bf16 v201, v133, v196, v201\n"
"v_dot2_f32_bf16 v205, v133, v133, v205\n"
"v_dot2_f32_bf16 v202, v134, v196, v202\n"
"v_dot2_f32_bf16 v206, v134, v134, v206\n"
"v_dot2_f32_bf16 v203, v135, v196, v203\n"
"v_dot2_f32_bf16 v207, v135, v135, v207\n"
"v_dot2_f32_bf16 v200, v136, v196, v200\n"
"v_dot2_f32_bf16 v204, v136, v136, v204\n"
"v_dot2_f32_bf16 v201, v137, v196, v201\n"
"v_dot2_f32_bf16 v205, v137, v137, v205\n"
"v_dot2_f32_bf16 v202, v138, v196, v202\n"
"v_dot2_f32_bf16 v206, v138, v138, v206\n"
"v_dot2_f32_bf16 v203, v139, v196, v203\n"
"v_dot2_f32_bf16 v207, v139, v139, v207\n"
"v_dot2_f32_bf16 v200, v140, v196, v200\n"
"v_dot2_f32_bf16 v204, v140, v140, v204\n"
"v_dot2_f32_bf16 v201, v141, v196, v201\n"
"v_dot2_f32_bf16 v205, v141, v141, v205\n"
"v_dot2_f32_bf16 v202, v142, v196, v202\n"
"v_dot2_f32_bf16 v206, v142, v142, v206\n"
"v_dot2_f32_bf16 v203, v143, v196, v203\n"
"v_dot2_f32_bf16 v207, v143, v143, v207\n"
"s_nop 3\n"
"v_pk_add_f32 v[200:201], v[200:201], v[202:203]\n"
"v_pk_add_f32 v[204:205], v[204:205], v[206:207]\n"
"v_add_f32 v200, v200, v201\n"
"v_add_f32 v204, v204, v205\n"
"s_nop 1\n"
"v_add_f32_dpp v200, v200, v200 quad_perm:[1,0,3,2] row_mask:0xf bank_mask:0xf\n"
"v_add_f32_dpp v204, v204, v204 quad_perm:[1,0,3,2] row_mask:0xf bank_mask:0xf\n"
"s_nop 1\n"
"v_add_f32_dpp v200, v200, v200 quad_perm:[2,3,0,1] row_mask:0xf bank_mask:0xf\n"
"v_add_f32_dpp v204, v204, v204 quad_perm:[2,3,0,1] row_mask:0xf bank_mask:0xf\n"
"v_mov_b32 v54, 0x3acccccd\n"
"v_mov_b32 v55, 0x3727c5ac\n"
"v_mul_f32 v200, v200, v54\n"
"v_mul_f32 v204, v204, v54\n"
"v_fma_f32 v204, -v200, v200, v204\n"
"v_max_f32 v204, 0, v204\n"
"v_add_f32 v204, v204, v55\n"
"v_rsq_f32 v204, v204\n"
"v_lshlrev_b32 v52, 2, v46\n"
"v_add_u32 v53, 0, v52\n"
"ds_bpermute_b32 v124, v53, v200\n"
"ds_bpermute_b32 v128, v53, v204\n"
"v_add_u32 v53, 16, v52\n"
"ds_bpermute_b32 v125, v53, v200\n"
"ds_bpermute_b32 v129, v53, v204\n"
"v_add_u32 v53, 32, v52\n"
"ds_bpermute_b32 v126, v53, v200\n"
"ds_bpermute_b32 v130, v53, v204\n"
"v_add_u32 v53, 48, v52\n"
"ds_bpermute_b32 v127, v53, v200\n"
"ds_bpermute_b32 v131, v53, v204\n"
"s_waitcnt vmcnt(9)\n"
"v_mul_u32_u24 v52, 640, v246\n"
"v_lshlrev_b32 v53, 1, v245\n"
"v_add_u32 v52, v52, v53\n"
"s_mul_i32 s72, s78, 3072\n"
"s_add_u32 s72, s72, s96\n"
"s_add_u32 s72, s72, 98304\n"
"v_add_u32 v52, s72, v52\n"
"ds_read_u16 v144, v52\n"
"ds_read_u16 v145, v52 offset:160\n"
"ds_read_u16 v146, v52 offset:320\n"
"ds_read_u16 v147, v52 offset:480\n"
"ds_read_u16 v148, v52 offset:32\n"
"ds_read_u16 v149, v52 offset:192\n"
"ds_read_u16 v150, v52 offset:352\n"
"ds_read_u16 v151, v52 offset:512\n"
"s_waitcnt lgkmcnt(14)\n"
"ds_read_u16 v152, v52 offset:64\n"
"ds_read_u16 v153, v52 offset:224\n"
"s_waitcnt lgkmcnt(14)\n"
"ds_read_u16 v154, v52 offset:384\n"
"ds_read_u16 v155, v52 offset:544\n"
"s_waitcnt lgkmcnt(14)\n"
"ds_read_u16 v156, v52 offset:96\n"
"ds_read_u16 v157, v52 offset:256\n"
"s_waitcnt lgkmcnt(14)\n"
"ds_read_u16 v158, v52 offset:416\n"
"ds_read_u16 v159, v52 offset:576\n"
"s_waitcnt lgkmcnt(14)\n"
"ds_read_u16 v160, v52 offset:128\n"
"ds_read_u16 v161, v52 offset:288\n"
"s_waitcnt lgkmcnt(14)\n"
"ds_read_u16 v162, v52 offset:448\n"
"ds_read_u16 v163, v52 offset:608\n"
"s_waitcnt lgkmcnt(14)\n"
"s_waitcnt lgkmcnt(0)\n"
"v_lshl_or_b32 v32, v145, 16, v144\n"
"v_lshl_or_b32 v33, v147, 16, v146\n"
"v_lshl_or_b32 v34, v149, 16, v148\n"
"v_lshl_or_b32 v35, v151, 16, v150\n"
"v_lshl_or_b32 v36, v153, 16, v152\n"
"v_lshl_or_b32 v37, v155, 16, v154\n"
"v_lshl_or_b32 v38, v157, 16, v156\n"
"v_lshl_or_b32 v39, v159, 16, v158\n"
"v_lshl_or_b32 v40, v161, 16, v160\n"
"v_lshl_or_b32 v41, v163, 16, v162\n"
"v_lshlrev_b32 v64, 16, v144\n"
"v_lshlrev_b32 v65, 16, v145\n"
"v_lshlrev_b32 v66, 16, v146\n"
"v_lshlrev_b32 v67, 16, v147\n"
"v_lshlrev_b32 v68, 16, v148\n"
"v_lshlrev_b32 v69, 16, v149\n"
"v_lshlrev_b32 v70, 16, v150\n"
"v_lshlrev_b32 v71, 16, v151\n"
"v_lshlrev_b32 v72, 16, v152\n"
"v_lshlrev_b32 v73, 16, v153\n"
"v_lshlrev_b32 v74, 16, v154\n"
"v_lshlrev_b32 v75, 16, v155\n"
"v_lshlrev_b32 v76, 16, v156\n"
"v_lshlrev_b32 v77, 16, v157\n"
"v_lshlrev_b32 v78, 16, v158\n"
"v_lshlrev_b32 v79, 16, v159\n"
"v_lshlrev_b32 v80, 16, v160\n"
"v_lshlrev_b32 v81, 16, v161\n"
"v_lshlrev_b32 v82, 16, v162\n"
"v_lshlrev_b32 v83, 16, v163\n"
"s_mul_i32 s68, s77, 327680\n"
"v_pk_add_f32 v[64:65], v[64:65], v[124:125] neg_lo:[0,1] neg_hi:[0,1]\n"
"v_pk_add_f32 v[66:67], v[66:67], v[126:127] neg_lo:[0,1] neg_hi:[0,1]\n"
"v_pk_mul_f32 v[64:65], v[64:65], v[128:129]\n"
"v_pk_mul_f32 v[66:67], v[66:67], v[130:131]\n"
"v_cvt_pk_bf16_f32 v132, v64, v65\n"
"v_cvt_pk_bf16_f32 v133, v66, v67\n"
"v_pk_add_f32 v[68:69], v[68:69], v[124:125] neg_lo:[0,1] neg_hi:[0,1]\n"
"v_pk_add_f32 v[70:71], v[70:71], v[126:127] neg_lo:[0,1] neg_hi:[0,1]\n"
"v_pk_mul_f32 v[68:69], v[68:69], v[128:129]\n"
"v_pk_mul_f32 v[70:71], v[70:71], v[130:131]\n"
"v_cvt_pk_bf16_f32 v134, v68, v69\n"
"v_cvt_pk_bf16_f32 v135, v70, v71\n"
"v_pk_add_f32 v[72:73], v[72:73], v[124:125] neg_lo:[0,1] neg_hi:[0,1]\n"
"v_pk_add_f32 v[74:75], v[74:75], v[126:127] neg_lo:[0,1] neg_hi:[0,1]\n"
"v_pk_mul_f32 v[72:73], v[72:73], v[128:129]\n"
"v_pk_mul_f32 v[74:75], v[74:75], v[130:131]\n"
"v_cvt_pk_bf16_f32 v136, v72, v73\n"
"v_cvt_pk_bf16_f32 v137, v74, v75\n"
"v_pk_add_f32 v[76:77], v[76:77], v[124:125] neg_lo:[0,1] neg_hi:[0,1]\n"
"v_pk_add_f32 v[78:79], v[78:79], v[126:127] neg_lo:[0,1] neg_hi:[0,1]\n"
"v_pk_mul_f32 v[76:77], v[76:77], v[128:129]\n"
"v_pk_mul_f32 v[78:79], v[78:79], v[130:131]\n"
"v_cvt_pk_bf16_f32 v138, v76, v77\n"
"v_cvt_pk_bf16_f32 v139, v78, v79\n"
"v_pk_add_f32 v[80:81], v[80:81], v[124:125] neg_lo:[0,1] neg_hi:[0,1]\n"
"v_pk_add_f32 v[82:83], v[82:83], v[126:127] neg_lo:[0,1] neg_hi:[0,1]\n"
"v_pk_mul_f32 v[80:81], v[80:81], v[128:129]\n"
"v_pk_mul_f32 v[82:83], v[82:83], v[130:131]\n"
"v_cvt_pk_bf16_f32 v140, v80, v81\n"
"v_cvt_pk_bf16_f32 v141, v82, v83\n"
"s_nop 1\n"
"v_permlane16_swap_b32 v132, v134\n"
"v_permlane16_swap_b32 v133, v135\n"
"v_permlane16_swap_b32 v136, v138\n"
"v_permlane16_swap_b32 v137, v139\n"
"s_mul_i32 s73, s76, 10240\n"
"s_add_u32 s72, s68, s73\n"
"buffer_store_dwordx4 v[132:135], v43, s[40:43], s72 offen sc1\n"
"buffer_store_dwordx4 v[136:139], v43, s[40:43], s72 offen offset:512 sc1\n"
"buffer_store_dwordx2 v[140:141], v42, s[40:43], s72 offen offset:1024 sc1\n"
"s_waitcnt vmcnt(4)\n"
"v_lshlrev_b32 v53, 4, v245\n"
"v_lshlrev_b32 v54, 2, v246\n"
"v_add_u32 v53, v53, v54\n"
"v_xor_b32 v164, v180, v164\n"
"v_xor_b32 v165, v180, v165\n"
"v_xor_b32 v166, v181, v166\n"
"v_xor_b32 v167, v181, v167\n"
"ds_bpermute_b32 v208, v53, v164\n"
"ds_bpermute_b32 v209, v53, v165\n"
"ds_bpermute_b32 v210, v53, v166\n"
"ds_bpermute_b32 v211, v53, v167\n"
"v_xor_b32 v168, v182, v168\n"
"v_xor_b32 v169, v182, v169\n"
"v_xor_b32 v170, v183, v170\n"
"v_xor_b32 v171, v183, v171\n"
"ds_bpermute_b32 v212, v53, v168\n"
"ds_bpermute_b32 v213, v53, v169\n"
"ds_bpermute_b32 v214, v53, v170\n"
"ds_bpermute_b32 v215, v53, v171\n"
"v_xor_b32 v172, v184, v172\n"
"v_xor_b32 v173, v184, v173\n"
"v_xor_b32 v174, v185, v174\n"
"v_xor_b32 v175, v185, v175\n"
"ds_bpermute_b32 v216, v53, v172\n"
"ds_bpermute_b32 v217, v53, v173\n"
"ds_bpermute_b32 v218, v53, v174\n"
"ds_bpermute_b32 v219, v53, v175\n"
"v_xor_b32 v176, v186, v176\n"
"v_xor_b32 v177, v186, v177\n"
"v_xor_b32 v178, v187, v178\n"
"v_xor_b32 v179, v187, v179\n"
"ds_bpermute_b32 v220, v53, v176\n"
"ds_bpermute_b32 v221, v53, v177\n"
"ds_bpermute_b32 v222, v53, v178\n"
"ds_bpermute_b32 v223, v53, v179\n"
"s_waitcnt lgkmcnt(14)\n"
"s_waitcnt lgkmcnt(0)\n"
"v_add_u32 v52, v188, v30\n"
"buffer_store_dwordx4 v[208:211], v52, s[40:43], 0 offen sc1\n"
"v_add_u32 v52, v189, v30\n"
"buffer_store_dwordx4 v[212:215], v52, s[40:43], 0 offen sc1\n"
"v_add_u32 v52, v190, v30\n"
"buffer_store_dwordx4 v[216:219], v52, s[40:43], 0 offen sc1\n"
"v_add_u32 v52, v191, v30\n"
"s_add_u32 s72, s78, 12\n"
"s_lshl_b32 s72, s72, 5\n"
"s_add_u32 s72, s72, s76\n"
"s_cmp_gt_u32 s72, 399\n"
"s_cbranch_scc1 12f\n"
"buffer_store_dwordx4 v[220:223], v52, s[40:43], 0 offen sc1\n"
"12:\n"
"s_lshl_b32 s72, s76, 3\n"
"s_add_u32 s72, s72, s77\n"
"s_lshl_b32 s72, s72, 2\n"
"s_add_u32 s72, s72, 35782656\n"
"s_waitcnt vmcnt(0)\n"
"s_barrier\n"
"s_cmp_lg_u32 s78, 0\n"
"s_cbranch_scc1 8f\n"
"s_mov_b64 exec, 1\n"
"v_mov_b32 v52, s36\n"
"v_mov_b32 v53, 0\n"
"buffer_store_dword v52, v53, s[40:43], s72 offen sc1\n"
"s_mov_b64 exec, -1\n"
"8:\n"
"s_load_dwordx2 s[52:53], s[38:39], 40\n"
"s_waitcnt lgkmcnt(0)\n"
"s_add_u32 m0, s64, 0\n"
"s_nop 0\n"
"buffer_load_dwordx4 v16, s[44:47], s65 offen lds\n"
"buffer_load_dwordx4 v17, s[44:47], s65 offen offset:1024 lds\n"
"buffer_load_dwordx4 v18, s[44:47], s65 offen offset:2048 lds\n"
"buffer_load_dwordx4 v19, s[44:47], s65 offen offset:3072 lds\n"
"s_add_u32 m0, s64, 4096\n"
"s_nop 0\n"
"buffer_load_dwordx4 v16, s[48:51], s65 offen lds\n"
"buffer_load_dwordx4 v17, s[48:51], s65 offen offset:1024 lds\n"
"buffer_load_dwordx4 v18, s[48:51], s65 offen offset:2048 lds\n"
"buffer_load_dwordx4 v19, s[48:51], s65 offen offset:3072 lds\n"
"s_add_u32 s65, s65, 256\n"
"s_and_b32 s65, s65, 0xfff\n"
"s_add_u32 m0, s64, 8192\n"
"s_nop 0\n"
"buffer_load_dwordx4 v16, s[44:47], s65 offen lds\n"
"buffer_load_dwordx4 v17, s[44:47], s65 offen offset:1024 lds\n"
"buffer_load_dwordx4 v18, s[44:47], s65 offen offset:2048 lds\n"
"buffer_load_dwordx4 v19, s[44:47], s65 offen offset:3072 lds\n"
"s_add_u32 m0, s64, 12288\n"
"s_nop 0\n"
"buffer_load_dwordx4 v16, s[48:51], s65 offen lds\n"
"buffer_load_dwordx4 v17, s[48:51], s65 offen offset:1024 lds\n"
"buffer_load_dwordx4 v18, s[48:51], s65 offen offset:2048 lds\n"
"buffer_load_dwordx4 v19, s[48:51], s65 offen offset:3072 lds\n"
"s_add_u32 s65, s65, 256\n"
"s_and_b32 s65, s65, 0xfff\n"
"s_add_u32 m0, s64, 16384\n"
"s_nop 0\n"
"buffer_load_dwordx4 v16, s[44:47], s65 offen lds\n"
"buffer_load_dwordx4 v17, s[44:47], s65 offen offset:1024 lds\n"
"buffer_load_dwordx4 v18, s[44:47], s65 offen offset:2048 lds\n"
"buffer_load_dwordx4 v19, s[44:47], s65 offen offset:3072 lds\n"
"s_add_u32 m0, s64, 20480\n"
"s_nop 0\n"
"buffer_load_dwordx4 v16, s[48:51], s65 offen lds\n"
"buffer_load_dwordx4 v17, s[48:51], s65 offen offset:1024 lds\n"
"buffer_load_dwordx4 v18, s[48:51], s65 offen offset:2048 lds\n"
"buffer_load_dwordx4 v19, s[48:51], s65 offen offset:3072 lds\n"
"s_add_u32 s65, s65, 256\n"
"s_and_b32 s65, s65, 0xfff\n"
"s_waitcnt vmcnt(16)\n"
"ds_read_b128 a[0:3], v25\n"
"ds_read_b128 a[4:7], v25 offset:4096\n"
"ds_read_b128 a[8:11], v26\n"
"ds_read_b128 a[12:15], v26 offset:4096\n"
"ds_read_b128 a[16:19], v27\n"
"ds_read_b128 a[20:23], v27 offset:4096\n"
"ds_read_b128 a[24:27], v28\n"
"ds_read_b128 a[28:31], v28 offset:4096\n"
"s_waitcnt lgkmcnt(0)\n"
"s_add_u32 m0, s64, 0\n"
"s_nop 0\n"
"buffer_load_dwordx4 v16, s[44:47], s65 offen lds\n"
"buffer_load_dwordx4 v17, s[44:47], s65 offen offset:1024 lds\n"
"buffer_load_dwordx4 v18, s[44:47], s65 offen offset:2048 lds\n"
"buffer_load_dwordx4 v19, s[44:47], s65 offen offset:3072 lds\n"
"s_add_u32 m0, s64, 4096\n"
"s_nop 0\n"
"buffer_load_dwordx4 v16, s[48:51], s65 offen lds\n"
"buffer_load_dwordx4 v17, s[48:51], s65 offen offset:1024 lds\n"
"buffer_load_dwordx4 v18, s[48:51], s65 offen offset:2048 lds\n"
"buffer_load_dwordx4 v19, s[48:51], s65 offen offset:3072 lds\n"
"s_add_u32 s65, s65, 256\n"
"s_and_b32 s65, s65, 0xfff\n"
"s_waitcnt vmcnt(16)\n"
"ds_read_b128 a[32:35], v25 offset:8192\n"
"ds_read_b128 a[36:39], v25 offset:12288\n"
"ds_read_b128 a[40:43], v26 offset:8192\n"
"ds_read_b128 a[44:47], v26 offset:12288\n"
"ds_read_b128 a[48:51], v27 offset:8192\n"
"ds_read_b128 a[52:55], v27 offset:12288\n"
"ds_read_b128 a[56:59], v28 offset:8192\n"
"ds_read_b128 a[60:63], v28 offset:12288\n"
"s_waitcnt lgkmcnt(0)\n"
"s_add_u32 m0, s64, 8192\n"
"s_nop 0\n"
"buffer_load_dwordx4 v16, s[44:47], s65 offen lds\n"
"buffer_load_dwordx4 v17, s[44:47], s65 offen offset:1024 lds\n"
"buffer_load_dwordx4 v18, s[44:47], s65 offen offset:2048 lds\n"
"buffer_load_dwordx4 v19, s[44:47], s65 offen offset:3072 lds\n"
"s_add_u32 m0, s64, 12288\n"
"s_nop 0\n"
"buffer_load_dwordx4 v16, s[48:51], s65 offen lds\n"
"buffer_load_dwordx4 v17, s[48:51], s65 offen offset:1024 lds\n"
"buffer_load_dwordx4 v18, s[48:51], s65 offen offset:2048 lds\n"
"buffer_load_dwordx4 v19, s[48:51], s65 offen offset:3072 lds\n"
"s_add_u32 s65, s65, 256\n"
"s_and_b32 s65, s65, 0xfff\n"
"s_waitcnt vmcnt(16)\n"
"ds_read_b128 a[64:67], v25 offset:16384\n"
"ds_read_b128 a[68:71], v25 offset:20480\n"
"ds_read_b128 a[72:75], v26 offset:16384\n"
"ds_read_b128 a[76:79], v26 offset:20480\n"
"ds_read_b128 a[80:83], v27 offset:16384\n"
"ds_read_b128 a[84:87], v27 offset:20480\n"
"ds_read_b128 a[88:91], v28 offset:16384\n"
"ds_read_b128 a[92:95], v28 offset:20480\n"
"s_waitcnt lgkmcnt(0)\n"
"s_add_u32 m0, s64, 16384\n"
"s_nop 0\n"
"buffer_load_dwordx4 v16, s[44:47], s65 offen lds\n"
"buffer_load_dwordx4 v17, s[44:47], s65 offen offset:1024 lds\n"
"buffer_load_dwordx4 v18, s[44:47], s65 offen offset:2048 lds\n"
"buffer_load_dwordx4 v19, s[44:47], s65 offen offset:3072 lds\n"
"s_add_u32 m0, s64, 20480\n"
"s_nop 0\n"
"buffer_load_dwordx4 v16, s[48:51], s65 offen lds\n"
"buffer_load_dwordx4 v17, s[48:51], s65 offen offset:1024 lds\n"
"buffer_load_dwordx4 v18, s[48:51], s65 offen offset:2048 lds\n"
"buffer_load_dwordx4 v19, s[48:51], s65 offen offset:3072 lds\n"
"s_add_u32 s65, s65, 256\n"
"s_and_b32 s65, s65, 0xfff\n"
"s_waitcnt vmcnt(16)\n"
"ds_read_b128 a[96:99], v25\n"
"ds_read_b128 a[100:103], v25 offset:4096\n"
"ds_read_b128 a[104:107], v26\n"
"ds_read_b128 a[108:111], v26 offset:4096\n"
"ds_read_b128 a[112:115], v27\n"
"ds_read_b128 a[116:119], v27 offset:4096\n"
"ds_read_b128 a[120:123], v28\n"
"ds_read_b128 a[124:127], v28 offset:4096\n"
"s_waitcnt lgkmcnt(0)\n"
"s_add_u32 m0, s64, 0\n"
"s_nop 0\n"
"buffer_load_dwordx4 v16, s[44:47], s65 offen lds\n"
"buffer_load_dwordx4 v17, s[44:47], s65 offen offset:1024 lds\n"
"buffer_load_dwordx4 v18, s[44:47], s65 offen offset:2048 lds\n"
"buffer_load_dwordx4 v19, s[44:47], s65 offen offset:3072 lds\n"
"s_add_u32 m0, s64, 4096\n"
"s_nop 0\n"
"buffer_load_dwordx4 v16, s[48:51], s65 offen lds\n"
"buffer_load_dwordx4 v17, s[48:51], s65 offen offset:1024 lds\n"
"buffer_load_dwordx4 v18, s[48:51], s65 offen offset:2048 lds\n"
"buffer_load_dwordx4 v19, s[48:51], s65 offen offset:3072 lds\n"
"s_add_u32 s65, s65, 256\n"
"s_and_b32 s65, s65, 0xfff\n"
"s_waitcnt vmcnt(16)\n"
"ds_read_b128 a[128:131], v25 offset:8192\n"
"ds_read_b128 a[132:135], v25 offset:12288\n"
"ds_read_b128 a[136:139], v26 offset:8192\n"
"ds_read_b128 a[140:143], v26 offset:12288\n"
"ds_read_b128 a[144:147], v27 offset:8192\n"
"ds_read_b128 a[148:151], v27 offset:12288\n"
"ds_read_b128 a[152:155], v28 offset:8192\n"
"ds_read_b128 a[156:159], v28 offset:12288\n"
"s_waitcnt lgkmcnt(0)\n"
"s_add_u32 m0, s64, 8192\n"
"s_nop 0\n"
"buffer_load_dwordx4 v16, s[44:47], s65 offen lds\n"
"buffer_load_dwordx4 v17, s[44:47], s65 offen offset:1024 lds\n"
"buffer_load_dwordx4 v18, s[44:47], s65 offen offset:2048 lds\n"
"buffer_load_dwordx4 v19, s[44:47], s65 offen offset:3072 lds\n"
"s_add_u32 m0, s64, 12288\n"
"s_nop 0\n"
"buffer_load_dwordx4 v16, s[48:51], s65 offen lds\n"
"buffer_load_dwordx4 v17, s[48:51], s65 offen offset:1024 lds\n"
"buffer_load_dwordx4 v18, s[48:51], s65 offen offset:2048 lds\n"
"buffer_load_dwordx4 v19, s[48:51], s65 offen offset:3072 lds\n"
"s_add_u32 s65, s65, 256\n"
"s_and_b32 s65, s65, 0xfff\n"
"s_waitcnt vmcnt(16)\n"
"ds_read_b128 a[160:163], v25 offset:16384\n"
"ds_read_b128 a[164:167], v25 offset:20480\n"
"ds_read_b128 a[168:171], v26 offset:16384\n"
"ds_read_b128 a[172:175], v26 offset:20480\n"
"ds_read_b128 a[176:179], v27 offset:16384\n"
"ds_read_b128 a[180:183], v27 offset:20480\n"
"ds_read_b128 a[184:187], v28 offset:16384\n"
"ds_read_b128 a[188:191], v28 offset:20480\n"
"s_waitcnt lgkmcnt(0)\n"
"s_add_u32 m0, s64, 16384\n"
"s_nop 0\n"
"buffer_load_dwordx4 v16, s[44:47], s65 offen lds\n"
"buffer_load_dwordx4 v17, s[44:47], s65 offen offset:1024 lds\n"
"buffer_load_dwordx4 v18, s[44:47], s65 offen offset:2048 lds\n"
"buffer_load_dwordx4 v19, s[44:47], s65 offen offset:3072 lds\n"
"s_add_u32 m0, s64, 20480\n"
"s_nop 0\n"
"buffer_load_dwordx4 v16, s[48:51], s65 offen lds\n"
"buffer_load_dwordx4 v17, s[48:51], s65 offen offset:1024 lds\n"
"buffer_load_dwordx4 v18, s[48:51], s65 offen offset:2048 lds\n"
"buffer_load_dwordx4 v19, s[48:51], s65 offen offset:3072 lds\n"
"s_add_u32 s65, s65, 256\n"
"s_and_b32 s65, s65, 0xfff\n"
"s_cmp_lg_u32 s78, 0\n"
"s_cbranch_scc1 99f\n"
"s_mov_b32 s70, 65536\n"
"s_mov_b32 s72, 35782656\n"
"91:\n"
"buffer_load_dwordx4 v[52:55], v30, s[40:43], s72 offen sc1\n"
"s_waitcnt vmcnt(0)\n"
"v_cmp_ne_u32 vcc, s36, v52\n"
"v_cmp_ne_u32 s[94:95], s36, v53\n"
"s_nop 4\n"
"s_or_b64 vcc, vcc, s[94:95]\n"
"v_cmp_ne_u32 s[94:95], s36, v54\n"
"s_nop 4\n"
"s_or_b64 vcc, vcc, s[94:95]\n"
"v_cmp_ne_u32 s[94:95], s36, v55\n"
"s_nop 4\n"
"s_or_b64 vcc, vcc, s[94:95]\n"
"s_cbranch_vccz 99f\n"
"s_sleep 1\n"
"s_sub_u32 s70, s70, 1\n"
"s_cmp_lg_u32 s70, 0\n"
"s_cbranch_scc1 91b\n"
"s_mov_b32 s84, 1\n"
"99:\n"
"s_barrier\n"
"2:\n"
"s_mul_i32 s68, s71, 2621440\n"
"s_mul_i32 s72, s77, 327680\n"
"s_add_u32 s68, s68, s72\n"
"s_add_u32 s69, s68, 327680\n"
"s_lshr_b32 s72, s75, 8\n"
"s_mul_i32 s72, s72, 20480\n"
"s_add_u32 s67, s68, s72\n"
"s_add_u32 m0, s37, 0\n"
"s_nop 0\n"
"buffer_load_dwordx4 v20, s[40:43], s67 offen sc1 lds\n"
"buffer_load_dwordx4 v20, s[40:43], s67 offen offset:1024 sc1 lds\n"
"buffer_load_dwordx4 v20, s[40:43], s67 offen offset:2048 sc1 lds\n"
"buffer_load_dwordx4 v20, s[40:43], s67 offen offset:3072 sc1 lds\n"
"s_add_u32 m0, s37, 4096\n"
"s_nop 0\n"
"buffer_load_dwordx4 v21, s[40:43], s67 offen sc1 lds\n"
"s_add_u32 s67, s67, 20480\n"
"s_cmp_eq_u32 s67, s69\n"
"s_cselect_b32 s67, s68, s67\n"
"s_add_u32 m0, s37, 20480\n"
"s_nop 0\n"
"buffer_load_dwordx4 v20, s[40:43], s67 offen sc1 lds\n"
"buffer_load_dwordx4 v20, s[40:43], s67 offen offset:1024 sc1 lds\n"
"buffer_load_dwordx4 v20, s[40:43], s67 offen offset:2048 sc1 lds\n"
"buffer_load_dwordx4 v20, s[40:43], s67 offen offset:3072 sc1 lds\n"
"s_add_u32 m0, s37, 24576\n"
"s_nop 0\n"
"buffer_load_dwordx4 v21, s[40:43], s67 offen sc1 lds\n"
"s_add_u32 s67, s67, 20480\n"
"s_cmp_eq_u32 s67, s69\n"
"s_cselect_b32 s67, s68, s67\n"
"s_add_u32 m0, s37, 40960\n"
"s_nop 0\n"
"buffer_load_dwordx4 v20, s[40:43], s67 offen sc1 lds\n"
"buffer_load_dwordx4 v20, s[40:43], s67 offen offset:1024 sc1 lds\n"
"buffer_load_dwordx4 v20, s[40:43], s67 offen offset:2048 sc1 lds\n"
"buffer_load_dwordx4 v20, s[40:43], s67 offen offset:3072 sc1 lds\n"
"s_add_u32 m0, s37, 45056\n"
"s_nop 0\n"
"buffer_load_dwordx4 v21, s[40:43], s67 offen sc1 lds\n"
"s_add_u32 s67, s67, 20480\n"
"s_cmp_eq_u32 s67, s69\n"
"s_cselect_b32 s67, s68, s67\n"
"s_mul_i32 s79, s71, 5242880\n"
"s_add_u32 s79, s79, 10485760\n"
"s_and_b32 s72, s71, 1\n"
"s_mul_i32 s80, s72, 1638400\n"
"s_lshl_b32 s72, s77, 3\n"
"s_lshl_b32 s73, s78, 1\n"
"s_add_u32 s72, s72, s73\n"
"s_mul_i32 s72, s72, 25600\n"
"s_add_u32 s80, s80, s72\n"
"s_add_u32 s80, s80, 31457280\n"
"s_mul_i32 s81, s71, 262144\n"
"s_lshl_b32 s72, s76, 13\n"
"s_add_u32 s81, s81, s72\n"
"s_add_u32 s81, s81, 34734080\n"
"s_lshl_b32 s82, s71, 5\n"
"s_add_u32 s82, s82, s76\n"
"s_lshl_b32 s82, s82, 8\n"
"s_add_u32 s82, s82, 35520512\n"
"s_lshl_b32 s83, s71, 3\n"
"s_add_u32 s83, s83, s77\n"
"s_mul_i32 s83, s83, 256\n"
"s_add_u32 s83, s83, 35553280\n"
"s_add_u32 s73, s78, 1\n"
"s_and_b32 s73, s73, 3\n"
"s_mul_i32 s73, s73, 5120\n"
"s_lshl_b32 s72, s76, 3\n"
"s_lshl_b32 s74, s78, 1\n"
"s_add_u32 s72, s72, s74\n"
"s_mul_i32 s72, s72, 20480\n"
"s_add_u32 s72, s72, s73\n"
"s_add_u32 s56, s72, s79\n"
"s_add_u32 s57, s56, 20480\n"
"s_add_u32 s73, s78, 2\n"
"s_and_b32 s73, s73, 3\n"
"s_mul_i32 s73, s73, 5120\n"
"s_lshl_b32 s72, s76, 3\n"
"s_lshl_b32 s74, s78, 1\n"
"s_add_u32 s72, s72, s74\n"
"s_mul_i32 s72, s72, 20480\n"
"s_add_u32 s72, s72, s73\n"
"s_add_u32 s58, s72, s79\n"
"s_add_u32 s59, s58, 20480\n"
"s_add_u32 s73, s78, 3\n"
"s_and_b32 s73, s73, 3\n"
"s_mul_i32 s73, s73, 5120\n"
"s_lshl_b32 s72, s76, 3\n"
"s_lshl_b32 s74, s78, 1\n"
"s_add_u32 s72, s72, s74\n"
"s_mul_i32 s72, s72, 20480\n"
"s_add_u32 s72, s72, s73\n"
"s_add_u32 s60, s72, s79\n"
"s_add_u32 s61, s60, 20480\n"
"s_add_u32 s73, s78, 4\n"
"s_and_b32 s73, s73, 3\n"
"s_mul_i32 s73, s73, 5120\n"
"s_lshl_b32 s72, s76, 3\n"
"s_lshl_b32 s74, s78, 1\n"
"s_add_u32 s72, s72, s74\n"
"s_mul_i32 s72, s72, 20480\n"
"s_add_u32 s72, s72, s73\n"
"s_add_u32 s62, s72, s79\n"
"s_add_u32 s63, s62, 20480\n"
"v_mov_b32 v104, 0\n"
"v_mov_b32 v105, 0\n"
"v_mov_b32 v106, 0\n"
"v_mov_b32 v107, 0\n"
"v_mov_b32 v108, 0\n"
"v_mov_b32 v109, 0\n"
"v_mov_b32 v110, 0\n"
"v_mov_b32 v111, 0\n"
"v_mov_b32 v112, 0\n"
"v_mov_b32 v113, 0\n"
"v_mov_b32 v114, 0\n"
"v_mov_b32 v115, 0\n"
"v_mov_b32 v116, 0\n"
"v_mov_b32 v117, 0\n"
"v_mov_b32 v118, 0\n"
"v_mov_b32 v119, 0\n"
"v_mov_b32 v120, 0\n"
"v_mov_b32 v121, 0\n"
"v_mov_b32 v122, 0\n"
"v_mov_b32 v123, 0\n"
"v_mov_b32 v124, 0\n"
"v_mov_b32 v125, 0\n"
"v_mov_b32 v126, 0\n"
"v_mov_b32 v127, 0\n"
"v_mov_b32 v128, 0\n"
"v_mov_b32 v129, 0\n"
"v_mov_b32 v130, 0\n"
"v_mov_b32 v131, 0\n"
"v_mov_b32 v132, 0\n"
"v_mov_b32 v133, 0\n"
"v_mov_b32 v134, 0\n"
"v_mov_b32 v135, 0\n"
"v_mov_b32 v136, 0\n"
"v_mov_b32 v137, 0\n"
"v_mov_b32 v138, 0\n"
"v_mov_b32 v139, 0\n"
"v_mov_b32 v140, 0\n"
"v_mov_b32 v141, 0\n"
"v_mov_b32 v142, 0\n"
"v_mov_b32 v143, 0\n"
"s_waitcnt vmcnt(10)\n"
"s_barrier\n"
"ds_read_b128 v[64:67], v29\n"
"ds_read_b128 v[68:71], v29 offset:256\n"
"ds_read_b128 v[72:75], v29 offset:512\n"
"ds_read_b128 v[76:79], v29 offset:768\n"
"ds_read_b128 v[80:83], v29 offset:1024\n"
"s_waitcnt lgkmcnt(4)\n"
"v_mfma_f32_16x16x32_bf16 v[104:107], v[64:67], a[0:3], v[104:107]\n"
"ds_read_b128 v[84:87], v29 offset:5120\n"
"s_waitcnt lgkmcnt(4)\n"
"v_mfma_f32_16x16x32_bf16 v[108:111], v[68:71], a[0:3], v[108:111]\n"
"ds_read_b128 v[88:91], v29 offset:5376\n"
"s_waitcnt lgkmcnt(4)\n"
"v_mfma_f32_16x16x32_bf16 v[112:115], v[72:75], a[0:3], v[112:115]\n"
"ds_read_b128 v[92:95], v29 offset:5632\n"
"s_waitcnt lgkmcnt(4)\n"
"v_mfma_f32_16x16x32_bf16 v[116:119], v[76:79], a[0:3], v[116:119]\n"
"ds_read_b128 v[96:99], v29 offset:5888\n"
"s_waitcnt lgkmcnt(4)\n"
"v_mfma_f32_16x16x32_bf16 v[120:123], v[80:83], a[0:3], v[120:123]\n"
"ds_read_b128 v[100:103], v29 offset:6144\n"
"v_mfma_f32_16x16x32_bf16 v[124:127], v[64:67], a[4:7], v[124:127]\n"
"ds_read_b128 a[192:195], v25\n"
"v_mfma_f32_16x16x32_bf16 v[128:131], v[68:71], a[4:7], v[128:131]\n"
"ds_read_b128 a[196:199], v25 offset:4096\n"
"v_mfma_f32_16x16x32_bf16 v[132:135], v[72:75], a[4:7], v[132:135]\n"
"ds_read_b128 a[200:203], v26\n"
"v_mfma_f32_16x16x32_bf16 v[136:139], v[76:79], a[4:7], v[136:139]\n"
"v_mfma_f32_16x16x32_bf16 v[140:143], v[80:83], a[4:7], v[140:143]\n"
"s_waitcnt lgkmcnt(7)\n"
"v_mfma_f32_16x16x32_bf16 v[104:107], v[84:87], a[8:11], v[104:107]\n"
"ds_read_b128 v[64:67], v29 offset:10240\n"
"s_waitcnt lgkmcnt(7)\n"
"v_mfma_f32_16x16x32_bf16 v[108:111], v[88:91], a[8:11], v[108:111]\n"
"ds_read_b128 v[68:71], v29 offset:10496\n"
"s_waitcnt lgkmcnt(7)\n"
"v_mfma_f32_16x16x32_bf16 v[112:115], v[92:95], a[8:11], v[112:115]\n"
"ds_read_b128 v[72:75], v29 offset:10752\n"
"s_waitcnt lgkmcnt(7)\n"
"v_mfma_f32_16x16x32_bf16 v[116:119], v[96:99], a[8:11], v[116:119]\n"
"ds_read_b128 v[76:79], v29 offset:11008\n"
"s_waitcnt lgkmcnt(7)\n"
"v_mfma_f32_16x16x32_bf16 v[120:123], v[100:103], a[8:11], v[120:123]\n"
"ds_read_b128 v[80:83], v29 offset:11264\n"
"v_mfma_f32_16x16x32_bf16 v[124:127], v[84:87], a[12:15], v[124:127]\n"
"ds_read_b128 a[204:207], v26 offset:4096\n"
"v_mfma_f32_16x16x32_bf16 v[128:131], v[88:91], a[12:15], v[128:131]\n"
"ds_read_b128 a[208:211], v27\n"
"v_mfma_f32_16x16x32_bf16 v[132:135], v[92:95], a[12:15], v[132:135]\n"
"ds_read_b128 a[212:215], v27 offset:4096\n"
"v_mfma_f32_16x16x32_bf16 v[136:139], v[96:99], a[12:15], v[136:139]\n"
"v_mfma_f32_16x16x32_bf16 v[140:143], v[100:103], a[12:15], v[140:143]\n"
"s_waitcnt lgkmcnt(7)\n"
"v_mfma_f32_16x16x32_bf16 v[104:107], v[64:67], a[16:19], v[104:107]\n"
"ds_read_b128 v[84:87], v29 offset:15360\n"
"s_waitcnt lgkmcnt(7)\n"
"v_mfma_f32_16x16x32_bf16 v[108:111], v[68:71], a[16:19], v[108:111]\n"
"ds_read_b128 v[88:91], v29 offset:15616\n"
"s_waitcnt lgkmcnt(7)\n"
"v_mfma_f32_16x16x32_bf16 v[112:115], v[72:75], a[16:19], v[112:115]\n"
"ds_read_b128 v[92:95], v29 offset:15872\n"
"s_waitcnt lgkmcnt(7)\n"
"v_mfma_f32_16x16x32_bf16 v[116:119], v[76:79], a[16:19], v[116:119]\n"
"ds_read_b128 v[96:99], v29 offset:16128\n"
"s_waitcnt lgkmcnt(7)\n"
"v_mfma_f32_16x16x32_bf16 v[120:123], v[80:83], a[16:19], v[120:123]\n"
"ds_read_b128 v[100:103], v29 offset:16384\n"
"v_mfma_f32_16x16x32_bf16 v[124:127], v[64:67], a[20:23], v[124:127]\n"
"ds_read_b128 a[216:219], v28\n"
"v_mfma_f32_16x16x32_bf16 v[128:131], v[68:71], a[20:23], v[128:131]\n"
"ds_read_b128 a[220:223], v28 offset:4096\n"
"v_mfma_f32_16x16x32_bf16 v[132:135], v[72:75], a[20:23], v[132:135]\n"
"v_mfma_f32_16x16x32_bf16 v[136:139], v[76:79], a[20:23], v[136:139]\n"
"v_mfma_f32_16x16x32_bf16 v[140:143], v[80:83], a[20:23], v[140:143]\n"
"s_waitcnt lgkmcnt(6)\n"
"v_mfma_f32_16x16x32_bf16 v[104:107], v[84:87], a[24:27], v[104:107]\n"
"s_waitcnt lgkmcnt(5)\n"
"v_mfma_f32_16x16x32_bf16 v[108:111], v[88:91], a[24:27], v[108:111]\n"
"s_waitcnt lgkmcnt(4)\n"
"v_mfma_f32_16x16x32_bf16 v[112:115], v[92:95], a[24:27], v[112:115]\n"
"s_waitcnt lgkmcnt(0)\n"
"s_waitcnt vmcnt(5)\n"
"s_barrier\n"
"v_mfma_f32_16x16x32_bf16 v[116:119], v[96:99], a[24:27], v[116:119]\n"
"ds_read_b128 v[64:67], v29 offset:20480\n"
"s_add_u32 m0, s37, 0\n"
"s_nop 0\n"
"buffer_load_dwordx4 v20, s[40:43], s67 offen sc1 lds\n"
"v_mfma_f32_16x16x32_bf16 v[120:123], v[100:103], a[24:27], v[120:123]\n"
"ds_read_b128 v[68:71], v29 offset:20736\n"
"v_mfma_f32_16x16x32_bf16 v[124:127], v[84:87], a[28:31], v[124:127]\n"
"ds_read_b128 v[72:75], v29 offset:20992\n"
"v_mfma_f32_16x16x32_bf16 v[128:131], v[88:91], a[28:31], v[128:131]\n"
"ds_read_b128 v[76:79], v29 offset:21248\n"
"buffer_load_dwordx4 v20, s[40:43], s67 offen offset:1024 sc1 lds\n"
"v_mfma_f32_16x16x32_bf16 v[132:135], v[92:95], a[28:31], v[132:135]\n"
"ds_read_b128 v[80:83], v29 offset:21504\n"
"v_mfma_f32_16x16x32_bf16 v[136:139], v[96:99], a[28:31], v[136:139]\n"
"v_mfma_f32_16x16x32_bf16 v[140:143], v[100:103], a[28:31], v[140:143]\n"
"s_waitcnt lgkmcnt(4)\n"
"v_mfma_f32_16x16x32_bf16 v[104:107], v[64:67], a[32:35], v[104:107]\n"
"ds_read_b128 v[84:87], v29 offset:25600\n"
"buffer_load_dwordx4 v20, s[40:43], s67 offen offset:2048 sc1 lds\n"
"s_waitcnt lgkmcnt(4)\n"
"v_mfma_f32_16x16x32_bf16 v[108:111], v[68:71], a[32:35], v[108:111]\n"
"ds_read_b128 v[88:91], v29 offset:25856\n"
"s_waitcnt lgkmcnt(4)\n"
"v_mfma_f32_16x16x32_bf16 v[112:115], v[72:75], a[32:35], v[112:115]\n"
"ds_read_b128 v[92:95], v29 offset:26112\n"
"s_waitcnt lgkmcnt(4)\n"
"v_mfma_f32_16x16x32_bf16 v[116:119], v[76:79], a[32:35], v[116:119]\n"
"ds_read_b128 v[96:99], v29 offset:26368\n"
"s_waitcnt lgkmcnt(4)\n"
"v_mfma_f32_16x16x32_bf16 v[120:123], v[80:83], a[32:35], v[120:123]\n"
"ds_read_b128 v[100:103], v29 offset:26624\n"
"buffer_load_dwordx4 v20, s[40:43], s67 offen offset:3072 sc1 lds\n"
"v_mfma_f32_16x16x32_bf16 v[124:127], v[64:67], a[36:39], v[124:127]\n"
"ds_read_b128 a[224:227], v25 offset:8192\n"
"v_mfma_f32_16x16x32_bf16 v[128:131], v[68:71], a[36:39], v[128:131]\n"
"ds_read_b128 a[228:231], v25 offset:12288\n"
"s_add_u32 m0, s37, 4096\n"
"v_mfma_f32_16x16x32_bf16 v[132:135], v[72:75], a[36:39], v[132:135]\n"
"ds_read_b128 a[232:235], v26 offset:8192\n"
"buffer_load_dwordx4 v21, s[40:43], s67 offen sc1 lds\n"
"v_mfma_f32_16x16x32_bf16 v[136:139], v[76:79], a[36:39], v[136:139]\n"
"v_mfma_f32_16x16x32_bf16 v[140:143], v[80:83], a[36:39], v[140:143]\n"
"s_waitcnt lgkmcnt(7)\n"
"v_mfma_f32_16x16x32_bf16 v[104:107], v[84:87], a[40:43], v[104:107]\n"
"ds_read_b128 v[64:67], v29 offset:30720\n"
"s_add_u32 s67, s67, 20480\n"
"s_cmp_eq_u32 s67, s69\n"
"s_cselect_b32 s67, s68, s67\n"
"s_add_u32 m0, s64, 0\n"
"s_waitcnt lgkmcnt(7)\n"
"v_mfma_f32_16x16x32_bf16 v[108:111], v[88:91], a[40:43], v[108:111]\n"
"ds_read_b128 v[68:71], v29 offset:30976\n"
"buffer_load_dwordx4 v16, s[44:47], s65 offen lds\n"
"s_waitcnt lgkmcnt(7)\n"
"v_mfma_f32_16x16x32_bf16 v[112:115], v[92:95], a[40:43], v[112:115]\n"
"ds_read_b128 v[72:75], v29 offset:31232\n"
"s_waitcnt lgkmcnt(7)\n"
"v_mfma_f32_16x16x32_bf16 v[116:119], v[96:99], a[40:43], v[116:119]\n"
"ds_read_b128 v[76:79], v29 offset:31488\n"
"s_waitcnt lgkmcnt(7)\n"
"v_mfma_f32_16x16x32_bf16 v[120:123], v[100:103], a[40:43], v[120:123]\n"
"ds_read_b128 v[80:83], v29 offset:31744\n"
"s_add_u32 m0, s64, 0\n"
"v_mfma_f32_16x16x32_bf16 v[124:127], v[84:87], a[44:47], v[124:127]\n"
"ds_read_b128 a[236:239], v26 offset:12288\n"
"buffer_load_dwordx4 v17, s[44:47], s65 offen offset:1024 lds\n"
"v_mfma_f32_16x16x32_bf16 v[128:131], v[88:91], a[44:47], v[128:131]\n"
"ds_read_b128 a[240:243], v27 offset:8192\n"
"v_mfma_f32_16x16x32_bf16 v[132:135], v[92:95], a[44:47], v[132:135]\n"
"ds_read_b128 a[244:247], v27 offset:12288\n"
"s_add_u32 m0, s64, 0\n"
"v_mfma_f32_16x16x32_bf16 v[136:139], v[96:99], a[44:47], v[136:139]\n"
"buffer_load_dwordx4 v18, s[44:47], s65 offen offset:2048 lds\n"
"v_mfma_f32_16x16x32_bf16 v[140:143], v[100:103], a[44:47], v[140:143]\n"
"s_waitcnt lgkmcnt(7)\n"
"v_mfma_f32_16x16x32_bf16 v[104:107], v[64:67], a[48:51], v[104:107]\n"
"ds_read_b128 v[84:87], v29 offset:35840\n"
"s_waitcnt lgkmcnt(7)\n"
"v_mfma_f32_16x16x32_bf16 v[108:111], v[68:71], a[48:51], v[108:111]\n"
"ds_read_b128 v[88:91], v29 offset:36096\n"
"s_add_u32 m0, s64, 0\n"
"s_waitcnt lgkmcnt(7)\n"
"v_mfma_f32_16x16x32_bf16 v[112:115], v[72:75], a[48:51], v[112:115]\n"
"ds_read_b128 v[92:95], v29 offset:36352\n"
"buffer_load_dwordx4 v19, s[44:47], s65 offen offset:3072 lds\n"
"s_waitcnt lgkmcnt(7)\n"
"v_mfma_f32_16x16x32_bf16 v[116:119], v[76:79], a[48:51], v[116:119]\n"
"ds_read_b128 v[96:99], v29 offset:36608\n"
"s_waitcnt lgkmcnt(7)\n"
"v_mfma_f32_16x16x32_bf16 v[120:123], v[80:83], a[48:51], v[120:123]\n"
"ds_read_b128 v[100:103], v29 offset:36864\n"
"v_mfma_f32_16x16x32_bf16 v[124:127], v[64:67], a[52:55], v[124:127]\n"
"ds_read_b128 a[248:251], v28 offset:8192\n"
"s_add_u32 m0, s64, 4096\n"
"v_mfma_f32_16x16x32_bf16 v[128:131], v[68:71], a[52:55], v[128:131]\n"
"ds_read_b128 a[252:255], v28 offset:12288\n"
"buffer_load_dwordx4 v16, s[48:51], s65 offen lds\n"
"v_mfma_f32_16x16x32_bf16 v[132:135], v[72:75], a[52:55], v[132:135]\n"
"v_mfma_f32_16x16x32_bf16 v[136:139], v[76:79], a[52:55], v[136:139]\n"
"v_mfma_f32_16x16x32_bf16 v[140:143], v[80:83], a[52:55], v[140:143]\n"
"s_waitcnt lgkmcnt(6)\n"
"v_mfma_f32_16x16x32_bf16 v[104:107], v[84:87], a[56:59], v[104:107]\n"
"s_waitcnt lgkmcnt(5)\n"
"v_mfma_f32_16x16x32_bf16 v[108:111], v[88:91], a[56:59], v[108:111]\n"
"s_waitcnt lgkmcnt(4)\n"
"v_mfma_f32_16x16x32_bf16 v[112:115], v[92:95], a[56:59], v[112:115]\n"
"s_waitcnt lgkmcnt(0)\n"
"s_waitcnt vmcnt(10)\n"
"s_barrier\n"
"v_mfma_f32_16x16x32_bf16 v[116:119], v[96:99], a[56:59], v[116:119]\n"
"ds_read_b128 v[64:67], v51\n"
"v_mfma_f32_16x16x32_bf16 v[120:123], v[100:103], a[56:59], v[120:123]\n"
"ds_read_b128 v[68:71], v51 offset:256\n"
"v_mfma_f32_16x16x32_bf16 v[124:127], v[84:87], a[60:63], v[124:127]\n"
"ds_read_b128 v[72:75], v51 offset:512\n"
"v_mfma_f32_16x16x32_bf16 v[128:131], v[88:91], a[60:63], v[128:131]\n"
"ds_read_b128 v[76:79], v51 offset:768\n"
"v_mfma_f32_16x16x32_bf16 v[132:135], v[92:95], a[60:63], v[132:135]\n"
"ds_read_b128 v[80:83], v51 offset:1024\n"
"s_add_u32 m0, s37, 20480\n"
"v_mfma_f32_16x16x32_bf16 v[136:139], v[96:99], a[60:63], v[136:139]\n"
"buffer_load_dwordx4 v20, s[40:43], s67 offen sc1 lds\n"
"v_mfma_f32_16x16x32_bf16 v[140:143], v[100:103], a[60:63], v[140:143]\n"
"buffer_load_dwordx4 v20, s[40:43], s67 offen offset:1024 sc1 lds\n"
"s_waitcnt lgkmcnt(4)\n"
"v_mfma_f32_16x16x32_bf16 v[104:107], v[64:67], a[64:67], v[104:107]\n"
"ds_read_b128 v[84:87], v51 offset:5120\n"
"s_waitcnt lgkmcnt(4)\n"
"v_mfma_f32_16x16x32_bf16 v[108:111], v[68:71], a[64:67], v[108:111]\n"
"ds_read_b128 v[88:91], v51 offset:5376\n"
"s_waitcnt lgkmcnt(4)\n"
"v_mfma_f32_16x16x32_bf16 v[112:115], v[72:75], a[64:67], v[112:115]\n"
"ds_read_b128 v[92:95], v51 offset:5632\n"
"s_waitcnt lgkmcnt(4)\n"
"v_mfma_f32_16x16x32_bf16 v[116:119], v[76:79], a[64:67], v[116:119]\n"
"ds_read_b128 v[96:99], v51 offset:5888\n"
"s_waitcnt lgkmcnt(4)\n"
"v_mfma_f32_16x16x32_bf16 v[120:123], v[80:83], a[64:67], v[120:123]\n"
"ds_read_b128 v[100:103], v51 offset:6144\n"
"v_mfma_f32_16x16x32_bf16 v[124:127], v[64:67], a[68:71], v[124:127]\n"
"v_mfma_f32_16x16x32_bf16 v[128:131], v[68:71], a[68:71], v[128:131]\n"
"buffer_load_dwordx4 v20, s[40:43], s67 offen offset:2048 sc1 lds\n"
"v_mfma_f32_16x16x32_bf16 v[132:135], v[72:75], a[68:71], v[132:135]\n"
"v_mfma_f32_16x16x32_bf16 v[136:139], v[76:79], a[68:71], v[136:139]\n"
"buffer_load_dwordx4 v20, s[40:43], s67 offen offset:3072 sc1 lds\n"
"s_add_u32 m0, s37, 24576\n"
"v_mfma_f32_16x16x32_bf16 v[140:143], v[80:83], a[68:71], v[140:143]\n"
"buffer_load_dwordx4 v21, s[40:43], s67 offen sc1 lds\n"
"s_waitcnt lgkmcnt(4)\n"
"v_mfma_f32_16x16x32_bf16 v[104:107], v[84:87], a[72:75], v[104:107]\n"
"ds_read_b128 v[64:67], v51 offset:10240\n"
"s_waitcnt lgkmcnt(4)\n"
"v_mfma_f32_16x16x32_bf16 v[108:111], v[88:91], a[72:75], v[108:111]\n"
"ds_read_b128 v[68:71], v51 offset:10496\n"
"s_waitcnt lgkmcnt(4)\n"
"v_mfma_f32_16x16x32_bf16 v[112:115], v[92:95], a[72:75], v[112:115]\n"
"ds_read_b128 v[72:75], v51 offset:10752\n"
"s_waitcnt lgkmcnt(4)\n"
"v_mfma_f32_16x16x32_bf16 v[116:119], v[96:99], a[72:75], v[116:119]\n"
"ds_read_b128 v[76:79], v51 offset:11008\n"
"s_waitcnt lgkmcnt(4)\n"
"v_mfma_f32_16x16x32_bf16 v[120:123], v[100:103], a[72:75], v[120:123]\n"
"ds_read_b128 v[80:83], v51 offset:11264\n"
"v_mfma_f32_16x16x32_bf16 v[124:127], v[84:87], a[76:79], v[124:127]\n"
"s_add_u32 s67, s67, 20480\n"
"s_cmp_eq_u32 s67, s69\n"
"s_cselect_b32 s67, s68, s67\n"
"s_add_u32 m0, s64, 4096\n"
"v_mfma_f32_16x16x32_bf16 v[128:131], v[88:91], a[76:79], v[128:131]\n"
"buffer_load_dwordx4 v17, s[48:51], s65 offen offset:1024 lds\n"
"v_mfma_f32_16x16x32_bf16 v[132:135], v[92:95], a[76:79], v[132:135]\n"
"s_add_u32 m0, s64, 4096\n"
"v_mfma_f32_16x16x32_bf16 v[136:139], v[96:99], a[76:79], v[136:139]\n"
"buffer_load_dwordx4 v18, s[48:51], s65 offen offset:2048 lds\n"
"s_add_u32 m0, s64, 4096\n"
"v_mfma_f32_16x16x32_bf16 v[140:143], v[100:103], a[76:79], v[140:143]\n"
"buffer_load_dwordx4 v19, s[48:51], s65 offen offset:3072 lds\n"
"s_waitcnt lgkmcnt(4)\n"
"v_mfma_f32_16x16x32_bf16 v[104:107], v[64:67], a[80:83], v[104:107]\n"
"ds_read_b128 v[84:87], v51 offset:15360\n"
"s_waitcnt lgkmcnt(4)\n"
"v_mfma_f32_16x16x32_bf16 v[108:111], v[68:71], a[80:83], v[108:111]\n"
"ds_read_b128 v[88:91], v51 offset:15616\n"
"s_waitcnt lgkmcnt(4)\n"
"v_mfma_f32_16x16x32_bf16 v[112:115], v[72:75], a[80:83], v[112:115]\n"
"ds_read_b128 v[92:95], v51 offset:15872\n"
"s_waitcnt lgkmcnt(4)\n"
"v_mfma_f32_16x16x32_bf16 v[116:119], v[76:79], a[80:83], v[116:119]\n"
"ds_read_b128 v[96:99], v51 offset:16128\n"
"s_waitcnt lgkmcnt(4)\n"
"v_mfma_f32_16x16x32_bf16 v[120:123], v[80:83], a[80:83], v[120:123]\n"
"ds_read_b128 v[100:103], v51 offset:16384\n"
"v_mfma_f32_16x16x32_bf16 v[124:127], v[64:67], a[84:87], v[124:127]\n"
"s_add_u32 s65, s65, 256\n"
"s_and_b32 s65, s65, 0xfff\n"
"s_add_u32 m0, s64, 8192\n"
"v_mfma_f32_16x16x32_bf16 v[128:131], v[68:71], a[84:87], v[128:131]\n"
"buffer_load_dwordx4 v16, s[44:47], s65 offen lds\n"
"v_mfma_f32_16x16x32_bf16 v[132:135], v[72:75], a[84:87], v[132:135]\n"
"s_add_u32 m0, s64, 8192\n"
"v_mfma_f32_16x16x32_bf16 v[136:139], v[76:79], a[84:87], v[136:139]\n"
"buffer_load_dwordx4 v17, s[44:47], s65 offen offset:1024 lds\n"
"v_mfma_f32_16x16x32_bf16 v[140:143], v[80:83], a[84:87], v[140:143]\n"
"s_waitcnt lgkmcnt(4)\n"
"v_mfma_f32_16x16x32_bf16 v[104:107], v[84:87], a[88:91], v[104:107]\n"
"s_waitcnt lgkmcnt(3)\n"
"v_mfma_f32_16x16x32_bf16 v[108:111], v[88:91], a[88:91], v[108:111]\n"
"s_waitcnt lgkmcnt(2)\n"
"v_mfma_f32_16x16x32_bf16 v[112:115], v[92:95], a[88:91], v[112:115]\n"
"s_waitcnt lgkmcnt(0)\n"
"s_waitcnt vmcnt(15)\n"
"s_barrier\n"
"v_mfma_f32_16x16x32_bf16 v[116:119], v[96:99], a[88:91], v[116:119]\n"
"ds_read_b128 v[64:67], v29\n"
"v_mfma_f32_16x16x32_bf16 v[120:123], v[100:103], a[88:91], v[120:123]\n"
"ds_read_b128 v[68:71], v29 offset:256\n"
"v_mfma_f32_16x16x32_bf16 v[124:127], v[84:87], a[92:95], v[124:127]\n"
"ds_read_b128 v[72:75], v29 offset:512\n"
"v_mfma_f32_16x16x32_bf16 v[128:131], v[88:91], a[92:95], v[128:131]\n"
"ds_read_b128 v[76:79], v29 offset:768\n"
"v_mfma_f32_16x16x32_bf16 v[132:135], v[92:95], a[92:95], v[132:135]\n"
"ds_read_b128 v[80:83], v29 offset:1024\n"
"s_add_u32 m0, s37, 40960\n"
"v_mfma_f32_16x16x32_bf16 v[136:139], v[96:99], a[92:95], v[136:139]\n"
"buffer_load_dwordx4 v20, s[40:43], s67 offen sc1 lds\n"
"v_mfma_f32_16x16x32_bf16 v[140:143], v[100:103], a[92:95], v[140:143]\n"
"buffer_load_dwordx4 v20, s[40:43], s67 offen offset:1024 sc1 lds\n"
"s_waitcnt lgkmcnt(4)\n"
"v_mfma_f32_16x16x32_bf16 v[104:107], v[64:67], a[96:99], v[104:107]\n"
"ds_read_b128 v[84:87], v29 offset:5120\n"
"s_waitcnt lgkmcnt(4)\n"
"v_mfma_f32_16x16x32_bf16 v[108:111], v[68:71], a[96:99], v[108:111]\n"
"ds_read_b128 v[88:91], v29 offset:5376\n"
"s_waitcnt lgkmcnt(4)\n"
"v_mfma_f32_16x16x32_bf16 v[112:115], v[72:75], a[96:99], v[112:115]\n"
"ds_read_b128 v[92:95], v29 offset:5632\n"
"s_waitcnt lgkmcnt(4)\n"
"v_mfma_f32_16x16x32_bf16 v[116:119], v[76:79], a[96:99], v[116:119]\n"
"ds_read_b128 v[96:99], v29 offset:5888\n"
"s_waitcnt lgkmcnt(4)\n"
"v_mfma_f32_16x16x32_bf16 v[120:123], v[80:83], a[96:99], v[120:123]\n"
"ds_read_b128 v[100:103], v29 offset:6144\n"
"v_mfma_f32_16x16x32_bf16 v[124:127], v[64:67], a[100:103], v[124:127]\n"
"ds_read_b128 a[0:3], v25 offset:16384\n"
"v_mfma_f32_16x16x32_bf16 v[128:131], v[68:71], a[100:103], v[128:131]\n"
"ds_read_b128 a[4:7], v25 offset:20480\n"
"v_mfma_f32_16x16x32_bf16 v[132:135], v[72:75], a[100:103], v[132:135]\n"
"ds_read_b128 a[8:11], v26 offset:16384\n"
"v_mfma_f32_16x16x32_bf16 v[136:139], v[76:79], a[100:103], v[136:139]\n"
"buffer_load_dwordx4 v20, s[40:43], s67 offen offset:2048 sc1 lds\n"
"v_mfma_f32_16x16x32_bf16 v[140:143], v[80:83], a[100:103], v[140:143]\n"
"buffer_load_dwordx4 v20, s[40:43], s67 offen offset:3072 sc1 lds\n"
"s_waitcnt lgkmcnt(7)\n"
"v_mfma_f32_16x16x32_bf16 v[104:107], v[84:87], a[104:107], v[104:107]\n"
"ds_read_b128 v[64:67], v29 offset:10240\n"
"s_waitcnt lgkmcnt(7)\n"
"v_mfma_f32_16x16x32_bf16 v[108:111], v[88:91], a[104:107], v[108:111]\n"
"ds_read_b128 v[68:71], v29 offset:10496\n"
"s_waitcnt lgkmcnt(7)\n"
"v_mfma_f32_16x16x32_bf16 v[112:115], v[92:95], a[104:107], v[112:115]\n"
"ds_read_b128 v[72:75], v29 offset:10752\n"
"s_waitcnt lgkmcnt(7)\n"
"v_mfma_f32_16x16x32_bf16 v[116:119], v[96:99], a[104:107], v[116:119]\n"
"ds_read_b128 v[76:79], v29 offset:11008\n"
"s_waitcnt lgkmcnt(7)\n"
"v_mfma_f32_16x16x32_bf16 v[120:123], v[100:103], a[104:107], v[120:123]\n"
"ds_read_b128 v[80:83], v29 offset:11264\n"
"v_mfma_f32_16x16x32_bf16 v[124:127], v[84:87], a[108:111], v[124:127]\n"
"ds_read_b128 a[12:15], v26 offset:20480\n"
"v_mfma_f32_16x16x32_bf16 v[128:131], v[88:91], a[108:111], v[128:131]\n"
"ds_read_b128 a[16:19], v27 offset:16384\n"
"v_mfma_f32_16x16x32_bf16 v[132:135], v[92:95], a[108:111], v[132:135]\n"
"ds_read_b128 a[20:23], v27 offset:20480\n"
"s_add_u32 m0, s37, 45056\n"
"v_mfma_f32_16x16x32_bf16 v[136:139], v[96:99], a[108:111], v[136:139]\n"
"buffer_load_dwordx4 v21, s[40:43], s67 offen sc1 lds\n"
"s_add_u32 s67, s67, 20480\n"
"s_cmp_eq_u32 s67, s69\n"
"s_cselect_b32 s67, s68, s67\n"
"s_add_u32 m0, s64, 8192\n"
"v_mfma_f32_16x16x32_bf16 v[140:143], v[100:103], a[108:111], v[140:143]\n"
"buffer_load_dwordx4 v18, s[44:47], s65 offen offset:2048 lds\n"
"s_waitcnt lgkmcnt(7)\n"
"v_mfma_f32_16x16x32_bf16 v[104:107], v[64:67], a[112:115], v[104:107]\n"
"ds_read_b128 v[84:87], v29 offset:15360\n"
"s_waitcnt lgkmcnt(7)\n"
"v_mfma_f32_16x16x32_bf16 v[108:111], v[68:71], a[112:115], v[108:111]\n"
"ds_read_b128 v[88:91], v29 offset:15616\n"
"s_waitcnt lgkmcnt(7)\n"
"v_mfma_f32_16x16x32_bf16 v[112:115], v[72:75], a[112:115], v[112:115]\n"
"ds_read_b128 v[92:95], v29 offset:15872\n"
"s_waitcnt lgkmcnt(7)\n"
"v_mfma_f32_16x16x32_bf16 v[116:119], v[76:79], a[112:115], v[116:119]\n"
"ds_read_b128 v[96:99], v29 offset:16128\n"
"s_waitcnt lgkmcnt(7)\n"
"v_mfma_f32_16x16x32_bf16 v[120:123], v[80:83], a[112:115], v[120:123]\n"
"ds_read_b128 v[100:103], v29 offset:16384\n"
"v_mfma_f32_16x16x32_bf16 v[124:127], v[64:67], a[116:119], v[124:127]\n"
"ds_read_b128 a[24:27], v28 offset:16384\n"
"v_mfma_f32_16x16x32_bf16 v[128:131], v[68:71], a[116:119], v[128:131]\n"
"ds_read_b128 a[28:31], v28 offset:20480\n"
"s_add_u32 m0, s64, 8192\n"
"v_mfma_f32_16x16x32_bf16 v[132:135], v[72:75], a[116:119], v[132:135]\n"
"buffer_load_dwordx4 v19, s[44:47], s65 offen offset:3072 lds\n"
"s_add_u32 m0, s64, 12288\n"
"v_mfma_f32_16x16x32_bf16 v[136:139], v[76:79], a[116:119], v[136:139]\n"
"buffer_load_dwordx4 v16, s[48:51], s65 offen lds\n"
"s_add_u32 m0, s64, 12288\n"
"v_mfma_f32_16x16x32_bf16 v[140:143], v[80:83], a[116:119], v[140:143]\n"
"buffer_load_dwordx4 v17, s[48:51], s65 offen offset:1024 lds\n"
"s_waitcnt lgkmcnt(6)\n"
"v_mfma_f32_16x16x32_bf16 v[104:107], v[84:87], a[120:123], v[104:107]\n"
"s_waitcnt lgkmcnt(5)\n"
"v_mfma_f32_16x16x32_bf16 v[108:111], v[88:91], a[120:123], v[108:111]\n"
"s_waitcnt lgkmcnt(4)\n"
"v_mfma_f32_16x16x32_bf16 v[112:115], v[92:95], a[120:123], v[112:115]\n"
"s_waitcnt lgkmcnt(0)\n"
"s_waitcnt vmcnt(14)\n"
"s_barrier\n"
"v_mfma_f32_16x16x32_bf16 v[116:119], v[96:99], a[120:123], v[116:119]\n"
"ds_read_b128 v[64:67], v29 offset:20480\n"
"v_mfma_f32_16x16x32_bf16 v[120:123], v[100:103], a[120:123], v[120:123]\n"
"ds_read_b128 v[68:71], v29 offset:20736\n"
"v_mfma_f32_16x16x32_bf16 v[124:127], v[84:87], a[124:127], v[124:127]\n"
"ds_read_b128 v[72:75], v29 offset:20992\n"
"v_mfma_f32_16x16x32_bf16 v[128:131], v[88:91], a[124:127], v[128:131]\n"
"ds_read_b128 v[76:79], v29 offset:21248\n"
"v_mfma_f32_16x16x32_bf16 v[132:135], v[92:95], a[124:127], v[132:135]\n"
"ds_read_b128 v[80:83], v29 offset:21504\n"
"s_add_u32 m0, s37, 0\n"
"v_mfma_f32_16x16x32_bf16 v[136:139], v[96:99], a[124:127], v[136:139]\n"
"buffer_load_dwordx4 v20, s[40:43], s67 offen sc1 lds\n"
"v_mfma_f32_16x16x32_bf16 v[140:143], v[100:103], a[124:127], v[140:143]\n"
"buffer_load_dwordx4 v20, s[40:43], s67 offen offset:1024 sc1 lds\n"
"s_waitcnt lgkmcnt(4)\n"
"v_mfma_f32_16x16x32_bf16 v[104:107], v[64:67], a[128:131], v[104:107]\n"
"ds_read_b128 v[84:87], v29 offset:25600\n"
"s_waitcnt lgkmcnt(4)\n"
"v_mfma_f32_16x16x32_bf16 v[108:111], v[68:71], a[128:131], v[108:111]\n"
"ds_read_b128 v[88:91], v29 offset:25856\n"
"s_waitcnt lgkmcnt(4)\n"
"v_mfma_f32_16x16x32_bf16 v[112:115], v[72:75], a[128:131], v[112:115]\n"
"ds_read_b128 v[92:95], v29 offset:26112\n"
"s_waitcnt lgkmcnt(4)\n"
"v_mfma_f32_16x16x32_bf16 v[116:119], v[76:79], a[128:131], v[116:119]\n"
"ds_read_b128 v[96:99], v29 offset:26368\n"
"s_waitcnt lgkmcnt(4)\n"
"v_mfma_f32_16x16x32_bf16 v[120:123], v[80:83], a[128:131], v[120:123]\n"
"ds_read_b128 v[100:103], v29 offset:26624\n"
"v_mfma_f32_16x16x32_bf16 v[124:127], v[64:67], a[132:135], v[124:127]\n"
"v_mfma_f32_16x16x32_bf16 v[128:131], v[68:71], a[132:135], v[128:131]\n"
"buffer_load_dwordx4 v20, s[40:43], s67 offen offset:2048 sc1 lds\n"
"v_mfma_f32_16x16x32_bf16 v[132:135], v[72:75], a[132:135], v[132:135]\n"
"v_mfma_f32_16x16x32_bf16 v[136:139], v[76:79], a[132:135], v[136:139]\n"
"buffer_load_dwordx4 v20, s[40:43], s67 offen offset:3072 sc1 lds\n"
"s_add_u32 m0, s37, 4096\n"
"v_mfma_f32_16x16x32_bf16 v[140:143], v[80:83], a[132:135], v[140:143]\n"
"buffer_load_dwordx4 v21, s[40:43], s67 offen sc1 lds\n"
"s_waitcnt lgkmcnt(4)\n"
"v_mfma_f32_16x16x32_bf16 v[104:107], v[84:87], a[136:139], v[104:107]\n"
"ds_read_b128 v[64:67], v29 offset:30720\n"
"s_waitcnt lgkmcnt(4)\n"
"v_mfma_f32_16x16x32_bf16 v[108:111], v[88:91], a[136:139], v[108:111]\n"
"ds_read_b128 v[68:71], v29 offset:30976\n"
"s_waitcnt lgkmcnt(4)\n"
"v_mfma_f32_16x16x32_bf16 v[112:115], v[92:95], a[136:139], v[112:115]\n"
"ds_read_b128 v[72:75], v29 offset:31232\n"
"s_waitcnt lgkmcnt(4)\n"
"v_mfma_f32_16x16x32_bf16 v[116:119], v[96:99], a[136:139], v[116:119]\n"
"ds_read_b128 v[76:79], v29 offset:31488\n"
"s_waitcnt lgkmcnt(4)\n"
"v_mfma_f32_16x16x32_bf16 v[120:123], v[100:103], a[136:139], v[120:123]\n"
"ds_read_b128 v[80:83], v29 offset:31744\n"
"v_mfma_f32_16x16x32_bf16 v[124:127], v[84:87], a[140:143], v[124:127]\n"
"s_add_u32 s67, s67, 20480\n"
"s_cmp_eq_u32 s67, s69\n"
"s_cselect_b32 s67, s68, s67\n"
"s_add_u32 m0, s64, 12288\n"
"v_mfma_f32_16x16x32_bf16 v[128:131], v[88:91], a[140:143], v[128:131]\n"
"buffer_load_dwordx4 v18, s[48:51], s65 offen offset:2048 lds\n"
"v_mfma_f32_16x16x32_bf16 v[132:135], v[92:95], a[140:143], v[132:135]\n"
"s_add_u32 m0, s64, 12288\n"
"v_mfma_f32_16x16x32_bf16 v[136:139], v[96:99], a[140:143], v[136:139]\n"
"buffer_load_dwordx4 v19, s[48:51], s65 offen offset:3072 lds\n"
"s_add_u32 s65, s65, 256\n"
"s_and_b32 s65, s65, 0xfff\n"
"s_add_u32 m0, s64, 16384\n"
"v_mfma_f32_16x16x32_bf16 v[140:143], v[100:103], a[140:143], v[140:143]\n"
"buffer_load_dwordx4 v16, s[44:47], s65 offen lds\n"
"s_waitcnt lgkmcnt(4)\n"
"v_mfma_f32_16x16x32_bf16 v[104:107], v[64:67], a[144:147], v[104:107]\n"
"ds_read_b128 v[84:87], v29 offset:35840\n"
"s_waitcnt lgkmcnt(4)\n"
"v_mfma_f32_16x16x32_bf16 v[108:111], v[68:71], a[144:147], v[108:111]\n"
"ds_read_b128 v[88:91], v29 offset:36096\n"
"s_waitcnt lgkmcnt(4)\n"
"v_mfma_f32_16x16x32_bf16 v[112:115], v[72:75], a[144:147], v[112:115]\n"
"ds_read_b128 v[92:95], v29 offset:36352\n"
"s_waitcnt lgkmcnt(4)\n"
"v_mfma_f32_16x16x32_bf16 v[116:119], v[76:79], a[144:147], v[116:119]\n"
"ds_read_b128 v[96:99], v29 offset:36608\n"
"s_waitcnt lgkmcnt(4)\n"
"v_mfma_f32_16x16x32_bf16 v[120:123], v[80:83], a[144:147], v[120:123]\n"
"ds_read_b128 v[100:103], v29 offset:36864\n"
"v_mfma_f32_16x16x32_bf16 v[124:127], v[64:67], a[148:151], v[124:127]\n"
"s_add_u32 m0, s64, 16384\n"
"v_mfma_f32_16x16x32_bf16 v[128:131], v[68:71], a[148:151], v[128:131]\n"
"buffer_load_dwordx4 v17, s[44:47], s65 offen offset:1024 lds\n"
"v_mfma_f32_16x16x32_bf16 v[132:135], v[72:75], a[148:151], v[132:135]\n"
"s_add_u32 m0, s64, 16384\n"
"v_mfma_f32_16x16x32_bf16 v[136:139], v[76:79], a[148:151], v[136:139]\n"
"buffer_load_dwordx4 v18, s[44:47], s65 offen offset:2048 lds\n"
"v_mfma_f32_16x16x32_bf16 v[140:143], v[80:83], a[148:151], v[140:143]\n"
"s_waitcnt lgkmcnt(4)\n"
"v_mfma_f32_16x16x32_bf16 v[104:107], v[84:87], a[152:155], v[104:107]\n"
"s_waitcnt lgkmcnt(3)\n"
"v_mfma_f32_16x16x32_bf16 v[108:111], v[88:91], a[152:155], v[108:111]\n"
"s_waitcnt lgkmcnt(2)\n"
"v_mfma_f32_16x16x32_bf16 v[112:115], v[92:95], a[152:155], v[112:115]\n"
"s_waitcnt lgkmcnt(0)\n"
"s_waitcnt vmcnt(14)\n"
"s_barrier\n"
"v_mfma_f32_16x16x32_bf16 v[116:119], v[96:99], a[152:155], v[116:119]\n"
"ds_read_b128 v[64:67], v51\n"
"s_add_u32 m0, s37, 20480\n"
"s_nop 0\n"
"buffer_load_dwordx4 v20, s[40:43], s67 offen sc1 lds\n"
"v_mfma_f32_16x16x32_bf16 v[120:123], v[100:103], a[152:155], v[120:123]\n"
"ds_read_b128 v[68:71], v51 offset:256\n"
"v_mfma_f32_16x16x32_bf16 v[124:127], v[84:87], a[156:159], v[124:127]\n"
"ds_read_b128 v[72:75], v51 offset:512\n"
"v_mfma_f32_16x16x32_bf16 v[128:131], v[88:91], a[156:159], v[128:131]\n"
"ds_read_b128 v[76:79], v51 offset:768\n"
"buffer_load_dwordx4 v20, s[40:43], s67 offen offset:1024 sc1 lds\n"
"v_mfma_f32_16x16x32_bf16 v[132:135], v[92:95], a[156:159], v[132:135]\n"
"ds_read_b128 v[80:83], v51 offset:1024\n"
"v_mfma_f32_16x16x32_bf16 v[136:139], v[96:99], a[156:159], v[136:139]\n"
"v_mfma_f32_16x16x32_bf16 v[140:143], v[100:103], a[156:159], v[140:143]\n"
"s_waitcnt lgkmcnt(4)\n"
"v_mfma_f32_16x16x32_bf16 v[104:107], v[64:67], a[160:163], v[104:107]\n"
"ds_read_b128 v[84:87], v51 offset:5120\n"
"buffer_load_dwordx4 v20, s[40:43], s67 offen offset:2048 sc1 lds\n"
"s_waitcnt lgkmcnt(4)\n"
"v_mfma_f32_16x16x32_bf16 v[108:111], v[68:71], a[160:163], v[108:111]\n"
"ds_read_b128 v[88:91], v51 offset:5376\n"
"s_waitcnt lgkmcnt(4)\n"
"v_mfma_f32_16x16x32_bf16 v[112:115], v[72:75], a[160:163], v[112:115]\n"
"ds_read_b128 v[92:95], v51 offset:5632\n"
"s_waitcnt lgkmcnt(4)\n"
"v_mfma_f32_16x16x32_bf16 v[116:119], v[76:79], a[160:163], v[116:119]\n"
"ds_read_b128 v[96:99], v51 offset:5888\n"
"s_waitcnt lgkmcnt(4)\n"
"v_mfma_f32_16x16x32_bf16 v[120:123], v[80:83], a[160:163], v[120:123]\n"
"ds_read_b128 v[100:103], v51 offset:6144\n"
"buffer_load_dwordx4 v20, s[40:43], s67 offen offset:3072 sc1 lds\n"
"v_mfma_f32_16x16x32_bf16 v[124:127], v[64:67], a[164:167], v[124:127]\n"
"ds_read_b128 a[32:35], v25\n"
"v_mfma_f32_16x16x32_bf16 v[128:131], v[68:71], a[164:167], v[128:131]\n"
"ds_read_b128 a[36:39], v25 offset:4096\n"
"s_add_u32 m0, s37, 24576\n"
"v_mfma_f32_16x16x32_bf16 v[132:135], v[72:75], a[164:167], v[132:135]\n"
"ds_read_b128 a[40:43], v26\n"
"buffer_load_dwordx4 v21, s[40:43], s67 offen sc1 lds\n"
"v_mfma_f32_16x16x32_bf16 v[136:139], v[76:79], a[164:167], v[136:139]\n"
"v_mfma_f32_16x16x32_bf16 v[140:143], v[80:83], a[164:167], v[140:143]\n"
"s_waitcnt lgkmcnt(7)\n"
"v_mfma_f32_16x16x32_bf16 v[104:107], v[84:87], a[168:171], v[104:107]\n"
"ds_read_b128 v[64:67], v51 offset:10240\n"
"s_add_u32 s67, s67, 20480\n"
"s_cmp_eq_u32 s67, s69\n"
"s_cselect_b32 s67, s68, s67\n"
"s_add_u32 m0, s64, 16384\n"
"s_waitcnt lgkmcnt(7)\n"
"v_mfma_f32_16x16x32_bf16 v[108:111], v[88:91], a[168:171], v[108:111]\n"
"ds_read_b128 v[68:71], v51 offset:10496\n"
"buffer_load_dwordx4 v19, s[44:47], s65 offen offset:3072 lds\n"
"s_waitcnt lgkmcnt(7)\n"
"v_mfma_f32_16x16x32_bf16 v[112:115], v[92:95], a[168:171], v[112:115]\n"
"ds_read_b128 v[72:75], v51 offset:10752\n"
"s_waitcnt lgkmcnt(7)\n"
"v_mfma_f32_16x16x32_bf16 v[116:119], v[96:99], a[168:171], v[116:119]\n"
"ds_read_b128 v[76:79], v51 offset:11008\n"
"s_waitcnt lgkmcnt(7)\n"
"v_mfma_f32_16x16x32_bf16 v[120:123], v[100:103], a[168:171], v[120:123]\n"
"ds_read_b128 v[80:83], v51 offset:11264\n"
"s_add_u32 m0, s64, 20480\n"
"v_mfma_f32_16x16x32_bf16 v[124:127], v[84:87], a[172:175], v[124:127]\n"
"ds_read_b128 a[44:47], v26 offset:4096\n"
"buffer_load_dwordx4 v16, s[48:51], s65 offen lds\n"
"v_mfma_f32_16x16x32_bf16 v[128:131], v[88:91], a[172:175], v[128:131]\n"
"ds_read_b128 a[48:51], v27\n"
"v_mfma_f32_16x16x32_bf16 v[132:135], v[92:95], a[172:175], v[132:135]\n"
"ds_read_b128 a[52:55], v27 offset:4096\n"
"s_add_u32 m0, s64, 20480\n"
"v_mfma_f32_16x16x32_bf16 v[136:139], v[96:99], a[172:175], v[136:139]\n"
"buffer_load_dwordx4 v17, s[48:51], s65 offen offset:1024 lds\n"
"v_mfma_f32_16x16x32_bf16 v[140:143], v[100:103], a[172:175], v[140:143]\n"
"s_waitcnt lgkmcnt(7)\n"
"v_mfma_f32_16x16x32_bf16 v[104:107], v[64:67], a[176:179], v[104:107]\n"
"ds_read_b128 v[84:87], v51 offset:15360\n"
"s_waitcnt lgkmcnt(7)\n"
"v_mfma_f32_16x16x32_bf16 v[108:111], v[68:71], a[176:179], v[108:111]\n"
"ds_read_b128 v[88:91], v51 offset:15616\n"
"s_add_u32 m0, s64, 20480\n"
"s_waitcnt lgkmcnt(7)\n"
"v_mfma_f32_16x16x32_bf16 v[112:115], v[72:75], a[176:179], v[112:115]\n"
"ds_read_b128 v[92:95], v51 offset:15872\n"
"buffer_load_dwordx4 v18, s[48:51], s65 offen offset:2048 lds\n"
"s_waitcnt lgkmcnt(7)\n"
"v_mfma_f32_16x16x32_bf16 v[116:119], v[76:79], a[176:179], v[116:119]\n"
"ds_read_b128 v[96:99], v51 offset:16128\n"
"s_waitcnt lgkmcnt(7)\n"
"v_mfma_f32_16x16x32_bf16 v[120:123], v[80:83], a[176:179], v[120:123]\n"
"ds_read_b128 v[100:103], v51 offset:16384\n"
"v_mfma_f32_16x16x32_bf16 v[124:127], v[64:67], a[180:183], v[124:127]\n"
"ds_read_b128 a[56:59], v28\n"
"s_add_u32 m0, s64, 20480\n"
"v_mfma_f32_16x16x32_bf16 v[128:131], v[68:71], a[180:183], v[128:131]\n"
"ds_read_b128 a[60:63], v28 offset:4096\n"
"buffer_load_dwordx4 v19, s[48:51], s65 offen offset:3072 lds\n"
"s_add_u32 s65, s65, 256\n"
"s_and_b32 s65, s65, 0xfff\n"
"v_mfma_f32_16x16x32_bf16 v[132:135], v[72:75], a[180:183], v[132:135]\n"
"v_mfma_f32_16x16x32_bf16 v[136:139], v[76:79], a[180:183], v[136:139]\n"
"v_mfma_f32_16x16x32_bf16 v[140:143], v[80:83], a[180:183], v[140:143]\n"
"s_waitcnt lgkmcnt(6)\n"
"v_mfma_f32_16x16x32_bf16 v[104:107], v[84:87], a[184:187], v[104:107]\n"
"s_waitcnt lgkmcnt(5)\n"
"v_mfma_f32_16x16x32_bf16 v[108:111], v[88:91], a[184:187], v[108:111]\n"
"s_waitcnt lgkmcnt(4)\n"
"v_mfma_f32_16x16x32_bf16 v[112:115], v[92:95], a[184:187], v[112:115]\n"
"s_waitcnt lgkmcnt(0)\n"
"s_waitcnt vmcnt(15)\n"
"s_barrier\n"
"v_mfma_f32_16x16x32_bf16 v[116:119], v[96:99], a[184:187], v[116:119]\n"
"ds_read_b128 v[64:67], v29\n"
"v_mfma_f32_16x16x32_bf16 v[120:123], v[100:103], a[184:187], v[120:123]\n"
"ds_read_b128 v[68:71], v29 offset:256\n"
"v_mfma_f32_16x16x32_bf16 v[124:127], v[84:87], a[188:191], v[124:127]\n"
"ds_read_b128 v[72:75], v29 offset:512\n"
"v_mfma_f32_16x16x32_bf16 v[128:131], v[88:91], a[188:191], v[128:131]\n"
"ds_read_b128 v[76:79], v29 offset:768\n"
"v_mfma_f32_16x16x32_bf16 v[132:135], v[92:95], a[188:191], v[132:135]\n"
"ds_read_b128 v[80:83], v29 offset:1024\n"
"s_add_u32 m0, s37, 40960\n"
"v_mfma_f32_16x16x32_bf16 v[136:139], v[96:99], a[188:191], v[136:139]\n"
"buffer_load_dwordx4 v20, s[40:43], s67 offen sc1 lds\n"
"v_mfma_f32_16x16x32_bf16 v[140:143], v[100:103], a[188:191], v[140:143]\n"
"buffer_load_dwordx4 v20, s[40:43], s67 offen offset:1024 sc1 lds\n"
"s_waitcnt vmcnt(15)\n"
"s_waitcnt lgkmcnt(4)\n"
"v_mfma_f32_16x16x32_bf16 v[104:107], v[64:67], a[192:195], v[104:107]\n"
"ds_read_b128 v[84:87], v29 offset:5120\n"
"s_waitcnt lgkmcnt(4)\n"
"v_mfma_f32_16x16x32_bf16 v[108:111], v[68:71], a[192:195], v[108:111]\n"
"ds_read_b128 v[88:91], v29 offset:5376\n"
"s_waitcnt lgkmcnt(4)\n"
"v_mfma_f32_16x16x32_bf16 v[112:115], v[72:75], a[192:195], v[112:115]\n"
"ds_read_b128 v[92:95], v29 offset:5632\n"
"s_waitcnt lgkmcnt(4)\n"
"v_mfma_f32_16x16x32_bf16 v[116:119], v[76:79], a[192:195], v[116:119]\n"
"ds_read_b128 v[96:99], v29 offset:5888\n"
"s_waitcnt lgkmcnt(4)\n"
"v_mfma_f32_16x16x32_bf16 v[120:123], v[80:83], a[192:195], v[120:123]\n"
"ds_read_b128 v[100:103], v29 offset:6144\n"
"v_mfma_f32_16x16x32_bf16 v[124:127], v[64:67], a[196:199], v[124:127]\n"
"ds_read_b128 a[64:67], v25 offset:8192\n"
"v_mfma_f32_16x16x32_bf16 v[128:131], v[68:71], a[196:199], v[128:131]\n"
"ds_read_b128 a[68:71], v25 offset:12288\n"
"v_mfma_f32_16x16x32_bf16 v[132:135], v[72:75], a[196:199], v[132:135]\n"
"ds_read_b128 a[72:75], v26 offset:8192\n"
"v_mfma_f32_16x16x32_bf16 v[136:139], v[76:79], a[196:199], v[136:139]\n"
"buffer_load_dwordx4 v20, s[40:43], s67 offen offset:2048 sc1 lds\n"
"v_mfma_f32_16x16x32_bf16 v[140:143], v[80:83], a[196:199], v[140:143]\n"
"buffer_load_dwordx4 v20, s[40:43], s67 offen offset:3072 sc1 lds\n"
"s_waitcnt lgkmcnt(7)\n"
"v_mfma_f32_16x16x32_bf16 v[104:107], v[84:87], a[200:203], v[104:107]\n"
"ds_read_b128 v[64:67], v29 offset:10240\n"
"s_waitcnt lgkmcnt(7)\n"
"v_mfma_f32_16x16x32_bf16 v[108:111], v[88:91], a[200:203], v[108:111]\n"
"ds_read_b128 v[68:71], v29 offset:10496\n"
"s_waitcnt lgkmcnt(7)\n"
"v_mfma_f32_16x16x32_bf16 v[112:115], v[92:95], a[200:203], v[112:115]\n"
"ds_read_b128 v[72:75], v29 offset:10752\n"
"s_waitcnt lgkmcnt(7)\n"
"v_mfma_f32_16x16x32_bf16 v[116:119], v[96:99], a[200:203], v[116:119]\n"
"ds_read_b128 v[76:79], v29 offset:11008\n"
"s_waitcnt lgkmcnt(7)\n"
"v_mfma_f32_16x16x32_bf16 v[120:123], v[100:103], a[200:203], v[120:123]\n"
"ds_read_b128 v[80:83], v29 offset:11264\n"
"v_mfma_f32_16x16x32_bf16 v[124:127], v[84:87], a[204:207], v[124:127]\n"
"ds_read_b128 a[76:79], v26 offset:12288\n"
"v_mfma_f32_16x16x32_bf16 v[128:131], v[88:91], a[204:207], v[128:131]\n"
"ds_read_b128 a[80:83], v27 offset:8192\n"
"v_mfma_f32_16x16x32_bf16 v[132:135], v[92:95], a[204:207], v[132:135]\n"
"ds_read_b128 a[84:87], v27 offset:12288\n"
"s_add_u32 m0, s37, 45056\n"
"v_mfma_f32_16x16x32_bf16 v[136:139], v[96:99], a[204:207], v[136:139]\n"
"buffer_load_dwordx4 v21, s[40:43], s67 offen sc1 lds\n"
"s_add_u32 s67, s67, 20480\n"
"s_cmp_eq_u32 s67, s69\n"
"s_cselect_b32 s67, s68, s67\n"
"s_add_u32 m0, s64, 0\n"
"v_mfma_f32_16x16x32_bf16 v[140:143], v[100:103], a[204:207], v[140:143]\n"
"buffer_load_dwordx4 v16, s[44:47], s65 offen lds\n"
"s_waitcnt lgkmcnt(7)\n"
"v_mfma_f32_16x16x32_bf16 v[104:107], v[64:67], a[208:211], v[104:107]\n"
"ds_read_b128 v[84:87], v29 offset:15360\n"
"s_waitcnt lgkmcnt(7)\n"
"v_mfma_f32_16x16x32_bf16 v[108:111], v[68:71], a[208:211], v[108:111]\n"
"ds_read_b128 v[88:91], v29 offset:15616\n"
"s_waitcnt lgkmcnt(7)\n"
"v_mfma_f32_16x16x32_bf16 v[112:115], v[72:75], a[208:211], v[112:115]\n"
"ds_read_b128 v[92:95], v29 offset:15872\n"
"s_waitcnt lgkmcnt(7)\n"
"v_mfma_f32_16x16x32_bf16 v[116:119], v[76:79], a[208:211], v[116:119]\n"
"ds_read_b128 v[96:99], v29 offset:16128\n"
"s_waitcnt lgkmcnt(7)\n"
"v_mfma_f32_16x16x32_bf16 v[120:123], v[80:83], a[208:211], v[120:123]\n"
"ds_read_b128 v[100:103], v29 offset:16384\n"
"v_mfma_f32_16x16x32_bf16 v[124:127], v[64:67], a[212:215], v[124:127]\n"
"ds_read_b128 a[88:91], v28 offset:8192\n"
"v_mfma_f32_16x16x32_bf16 v[128:131], v[68:71], a[212:215], v[128:131]\n"
"ds_read_b128 a[92:95], v28 offset:12288\n"
"s_add_u32 m0, s64, 0\n"
"v_mfma_f32_16x16x32_bf16 v[132:135], v[72:75], a[212:215], v[132:135]\n"
"buffer_load_dwordx4 v17, s[44:47], s65 offen offset:1024 lds\n"
"s_add_u32 m0, s64, 0\n"
"v_mfma_f32_16x16x32_bf16 v[136:139], v[76:79], a[212:215], v[136:139]\n"
"buffer_load_dwordx4 v18, s[44:47], s65 offen offset:2048 lds\n"
"s_add_u32 m0, s64, 0\n"
"v_mfma_f32_16x16x32_bf16 v[140:143], v[80:83], a[212:215], v[140:143]\n"
"buffer_load_dwordx4 v19, s[44:47], s65 offen offset:3072 lds\n"
"s_waitcnt lgkmcnt(6)\n"
"v_mfma_f32_16x16x32_bf16 v[104:107], v[84:87], a[216:219], v[104:107]\n"
"s_waitcnt lgkmcnt(5)\n"
"v_mfma_f32_16x16x32_bf16 v[108:111], v[88:91], a[216:219], v[108:111]\n"
"s_waitcnt lgkmcnt(4)\n"
"v_mfma_f32_16x16x32_bf16 v[112:115], v[92:95], a[216:219], v[112:115]\n"
"s_waitcnt lgkmcnt(0)\n"
"s_waitcnt vmcnt(14)\n"
"s_barrier\n"
"v_mfma_f32_16x16x32_bf16 v[116:119], v[96:99], a[216:219], v[116:119]\n"
"ds_read_b128 v[64:67], v29 offset:20480\n"
"v_mfma_f32_16x16x32_bf16 v[120:123], v[100:103], a[216:219], v[120:123]\n"
"ds_read_b128 v[68:71], v29 offset:20736\n"
"v_mfma_f32_16x16x32_bf16 v[124:127], v[84:87], a[220:223], v[124:127]\n"
"ds_read_b128 v[72:75], v29 offset:20992\n"
"v_mfma_f32_16x16x32_bf16 v[128:131], v[88:91], a[220:223], v[128:131]\n"
"ds_read_b128 v[76:79], v29 offset:21248\n"
"v_mfma_f32_16x16x32_bf16 v[132:135], v[92:95], a[220:223], v[132:135]\n"
"ds_read_b128 v[80:83], v29 offset:21504\n"
"s_add_u32 m0, s37, 0\n"
"v_mfma_f32_16x16x32_bf16 v[136:139], v[96:99], a[220:223], v[136:139]\n"
"buffer_load_dwordx4 v20, s[40:43], s67 offen sc1 lds\n"
"v_mfma_f32_16x16x32_bf16 v[140:143], v[100:103], a[220:223], v[140:143]\n"
"buffer_load_dwordx4 v20, s[40:43], s67 offen offset:1024 sc1 lds\n"
"s_waitcnt lgkmcnt(4)\n"
"v_mfma_f32_16x16x32_bf16 v[104:107], v[64:67], a[224:227], v[104:107]\n"
"ds_read_b128 v[84:87], v29 offset:25600\n"
"s_waitcnt lgkmcnt(4)\n"
"v_mfma_f32_16x16x32_bf16 v[108:111], v[68:71], a[224:227], v[108:111]\n"
"ds_read_b128 v[88:91], v29 offset:25856\n"
"s_waitcnt lgkmcnt(4)\n"
"v_mfma_f32_16x16x32_bf16 v[112:115], v[72:75], a[224:227], v[112:115]\n"
"ds_read_b128 v[92:95], v29 offset:26112\n"
"s_waitcnt lgkmcnt(4)\n"
"v_mfma_f32_16x16x32_bf16 v[116:119], v[76:79], a[224:227], v[116:119]\n"
"ds_read_b128 v[96:99], v29 offset:26368\n"
"s_waitcnt lgkmcnt(4)\n"
"v_mfma_f32_16x16x32_bf16 v[120:123], v[80:83], a[224:227], v[120:123]\n"
"ds_read_b128 v[100:103], v29 offset:26624\n"
"v_mfma_f32_16x16x32_bf16 v[124:127], v[64:67], a[228:231], v[124:127]\n"
"v_mfma_f32_16x16x32_bf16 v[128:131], v[68:71], a[228:231], v[128:131]\n"
"buffer_load_dwordx4 v20, s[40:43], s67 offen offset:2048 sc1 lds\n"
"v_mfma_f32_16x16x32_bf16 v[132:135], v[72:75], a[228:231], v[132:135]\n"
"v_mfma_f32_16x16x32_bf16 v[136:139], v[76:79], a[228:231], v[136:139]\n"
"buffer_load_dwordx4 v20, s[40:43], s67 offen offset:3072 sc1 lds\n"
"s_add_u32 m0, s37, 4096\n"
"v_mfma_f32_16x16x32_bf16 v[140:143], v[80:83], a[228:231], v[140:143]\n"
"buffer_load_dwordx4 v21, s[40:43], s67 offen sc1 lds\n"
"s_waitcnt lgkmcnt(4)\n"
"v_mfma_f32_16x16x32_bf16 v[104:107], v[84:87], a[232:235], v[104:107]\n"
"ds_read_b128 v[64:67], v29 offset:30720\n"
"s_waitcnt lgkmcnt(4)\n"
"v_mfma_f32_16x16x32_bf16 v[108:111], v[88:91], a[232:235], v[108:111]\n"
"ds_read_b128 v[68:71], v29 offset:30976\n"
"s_waitcnt lgkmcnt(4)\n"
"v_mfma_f32_16x16x32_bf16 v[112:115], v[92:95], a[232:235], v[112:115]\n"
"ds_read_b128 v[72:75], v29 offset:31232\n"
"s_waitcnt lgkmcnt(4)\n"
"v_mfma_f32_16x16x32_bf16 v[116:119], v[96:99], a[232:235], v[116:119]\n"
"ds_read_b128 v[76:79], v29 offset:31488\n"
"s_waitcnt lgkmcnt(4)\n"
"v_mfma_f32_16x16x32_bf16 v[120:123], v[100:103], a[232:235], v[120:123]\n"
"ds_read_b128 v[80:83], v29 offset:31744\n"
"v_mfma_f32_16x16x32_bf16 v[124:127], v[84:87], a[236:239], v[124:127]\n"
"s_add_u32 s67, s67, 20480\n"
"s_cmp_eq_u32 s67, s69\n"
"s_cselect_b32 s67, s68, s67\n"
"s_add_u32 m0, s64, 4096\n"
"v_mfma_f32_16x16x32_bf16 v[128:131], v[88:91], a[236:239], v[128:131]\n"
"buffer_load_dwordx4 v16, s[48:51], s65 offen lds\n"
"v_mfma_f32_16x16x32_bf16 v[132:135], v[92:95], a[236:239], v[132:135]\n"
"s_add_u32 m0, s64, 4096\n"
"v_mfma_f32_16x16x32_bf16 v[136:139], v[96:99], a[236:239], v[136:139]\n"
"buffer_load_dwordx4 v17, s[48:51], s65 offen offset:1024 lds\n"
"s_add_u32 m0, s64, 4096\n"
"v_mfma_f32_16x16x32_bf16 v[140:143], v[100:103], a[236:239], v[140:143]\n"
"buffer_load_dwordx4 v18, s[48:51], s65 offen offset:2048 lds\n"
"s_waitcnt lgkmcnt(4)\n"
"v_mfma_f32_16x16x32_bf16 v[104:107], v[64:67], a[240:243], v[104:107]\n"
"ds_read_b128 v[84:87], v29 offset:35840\n"
"s_waitcnt lgkmcnt(4)\n"
"v_mfma_f32_16x16x32_bf16 v[108:111], v[68:71], a[240:243], v[108:111]\n"
"ds_read_b128 v[88:91], v29 offset:36096\n"
"s_waitcnt lgkmcnt(4)\n"
"v_mfma_f32_16x16x32_bf16 v[112:115], v[72:75], a[240:243], v[112:115]\n"
"ds_read_b128 v[92:95], v29 offset:36352\n"
"s_waitcnt lgkmcnt(4)\n"
"v_mfma_f32_16x16x32_bf16 v[116:119], v[76:79], a[240:243], v[116:119]\n"
"ds_read_b128 v[96:99], v29 offset:36608\n"
"s_waitcnt lgkmcnt(4)\n"
"v_mfma_f32_16x16x32_bf16 v[120:123], v[80:83], a[240:243], v[120:123]\n"
"ds_read_b128 v[100:103], v29 offset:36864\n"
"v_mfma_f32_16x16x32_bf16 v[124:127], v[64:67], a[244:247], v[124:127]\n"
"s_add_u32 m0, s64, 4096\n"
"v_mfma_f32_16x16x32_bf16 v[128:131], v[68:71], a[244:247], v[128:131]\n"
"buffer_load_dwordx4 v19, s[48:51], s65 offen offset:3072 lds\n"
"v_mfma_f32_16x16x32_bf16 v[132:135], v[72:75], a[244:247], v[132:135]\n"
"s_add_u32 s65, s65, 256\n"
"s_and_b32 s65, s65, 0xfff\n"
"s_add_u32 m0, s64, 8192\n"
"v_mfma_f32_16x16x32_bf16 v[136:139], v[76:79], a[244:247], v[136:139]\n"
"buffer_load_dwordx4 v16, s[44:47], s65 offen lds\n"
"v_mfma_f32_16x16x32_bf16 v[140:143], v[80:83], a[244:247], v[140:143]\n"
"s_waitcnt lgkmcnt(4)\n"
"v_mfma_f32_16x16x32_bf16 v[104:107], v[84:87], a[248:251], v[104:107]\n"
"s_waitcnt lgkmcnt(3)\n"
"v_mfma_f32_16x16x32_bf16 v[108:111], v[88:91], a[248:251], v[108:111]\n"
"s_waitcnt lgkmcnt(2)\n"
"v_mfma_f32_16x16x32_bf16 v[112:115], v[92:95], a[248:251], v[112:115]\n"
"s_waitcnt lgkmcnt(0)\n"
"s_waitcnt vmcnt(14)\n"
"s_barrier\n"
"v_mfma_f32_16x16x32_bf16 v[116:119], v[96:99], a[248:251], v[116:119]\n"
"ds_read_b128 v[64:67], v51\n"
"s_add_u32 m0, s37, 20480\n"
"s_nop 0\n"
"buffer_load_dwordx4 v20, s[40:43], s67 offen sc1 lds\n"
"v_mfma_f32_16x16x32_bf16 v[120:123], v[100:103], a[248:251], v[120:123]\n"
"ds_read_b128 v[68:71], v51 offset:256\n"
"v_mfma_f32_16x16x32_bf16 v[124:127], v[84:87], a[252:255], v[124:127]\n"
"ds_read_b128 v[72:75], v51 offset:512\n"
"v_mfma_f32_16x16x32_bf16 v[128:131], v[88:91], a[252:255], v[128:131]\n"
"ds_read_b128 v[76:79], v51 offset:768\n"
"buffer_load_dwordx4 v20, s[40:43], s67 offen offset:1024 sc1 lds\n"
"v_mfma_f32_16x16x32_bf16 v[132:135], v[92:95], a[252:255], v[132:135]\n"
"ds_read_b128 v[80:83], v51 offset:1024\n"
"v_mfma_f32_16x16x32_bf16 v[136:139], v[96:99], a[252:255], v[136:139]\n"
"v_mfma_f32_16x16x32_bf16 v[140:143], v[100:103], a[252:255], v[140:143]\n"
"s_waitcnt lgkmcnt(4)\n"
"v_mfma_f32_16x16x32_bf16 v[104:107], v[64:67], a[0:3], v[104:107]\n"
"ds_read_b128 v[84:87], v51 offset:5120\n"
"buffer_load_dwordx4 v20, s[40:43], s67 offen offset:2048 sc1 lds\n"
"s_waitcnt lgkmcnt(4)\n"
"v_mfma_f32_16x16x32_bf16 v[108:111], v[68:71], a[0:3], v[108:111]\n"
"ds_read_b128 v[88:91], v51 offset:5376\n"
"s_waitcnt lgkmcnt(4)\n"
"v_mfma_f32_16x16x32_bf16 v[112:115], v[72:75], a[0:3], v[112:115]\n"
"ds_read_b128 v[92:95], v51 offset:5632\n"
"s_waitcnt lgkmcnt(4)\n"
"v_mfma_f32_16x16x32_bf16 v[116:119], v[76:79], a[0:3], v[116:119]\n"
"ds_read_b128 v[96:99], v51 offset:5888\n"
"s_waitcnt lgkmcnt(4)\n"
"v_mfma_f32_16x16x32_bf16 v[120:123], v[80:83], a[0:3], v[120:123]\n"
"ds_read_b128 v[100:103], v51 offset:6144\n"
"buffer_load_dwordx4 v20, s[40:43], s67 offen offset:3072 sc1 lds\n"
"v_mfma_f32_16x16x32_bf16 v[124:127], v[64:67], a[4:7], v[124:127]\n"
"ds_read_b128 a[96:99], v25 offset:16384\n"
"v_mfma_f32_16x16x32_bf16 v[128:131], v[68:71], a[4:7], v[128:131]\n"
"ds_read_b128 a[100:103], v25 offset:20480\n"
"s_add_u32 m0, s37, 24576\n"
"v_mfma_f32_16x16x32_bf16 v[132:135], v[72:75], a[4:7], v[132:135]\n"
"ds_read_b128 a[104:107], v26 offset:16384\n"
"buffer_load_dwordx4 v21, s[40:43], s67 offen sc1 lds\n"
"v_mfma_f32_16x16x32_bf16 v[136:139], v[76:79], a[4:7], v[136:139]\n"
"v_mfma_f32_16x16x32_bf16 v[140:143], v[80:83], a[4:7], v[140:143]\n"
"s_waitcnt lgkmcnt(7)\n"
"v_mfma_f32_16x16x32_bf16 v[104:107], v[84:87], a[8:11], v[104:107]\n"
"ds_read_b128 v[64:67], v51 offset:10240\n"
"s_add_u32 s67, s67, 20480\n"
"s_cmp_eq_u32 s67, s69\n"
"s_cselect_b32 s67, s68, s67\n"
"s_add_u32 m0, s64, 8192\n"
"s_waitcnt lgkmcnt(7)\n"
"v_mfma_f32_16x16x32_bf16 v[108:111], v[88:91], a[8:11], v[108:111]\n"
"ds_read_b128 v[68:71], v51 offset:10496\n"
"buffer_load_dwordx4 v17, s[44:47], s65 offen offset:1024 lds\n"
"s_waitcnt lgkmcnt(7)\n"
"v_mfma_f32_16x16x32_bf16 v[112:115], v[92:95], a[8:11], v[112:115]\n"
"ds_read_b128 v[72:75], v51 offset:10752\n"
"s_waitcnt lgkmcnt(7)\n"
"v_mfma_f32_16x16x32_bf16 v[116:119], v[96:99], a[8:11], v[116:119]\n"
"ds_read_b128 v[76:79], v51 offset:11008\n"
"s_waitcnt lgkmcnt(7)\n"
"v_mfma_f32_16x16x32_bf16 v[120:123], v[100:103], a[8:11], v[120:123]\n"
"ds_read_b128 v[80:83], v51 offset:11264\n"
"s_add_u32 m0, s64, 8192\n"
"v_mfma_f32_16x16x32_bf16 v[124:127], v[84:87], a[12:15], v[124:127]\n"
"ds_read_b128 a[108:111], v26 offset:20480\n"
"buffer_load_dwordx4 v18, s[44:47], s65 offen offset:2048 lds\n"
"v_mfma_f32_16x16x32_bf16 v[128:131], v[88:91], a[12:15], v[128:131]\n"
"ds_read_b128 a[112:115], v27 offset:16384\n"
"v_mfma_f32_16x16x32_bf16 v[132:135], v[92:95], a[12:15], v[132:135]\n"
"ds_read_b128 a[116:119], v27 offset:20480\n"
"s_add_u32 m0, s64, 8192\n"
"v_mfma_f32_16x16x32_bf16 v[136:139], v[96:99], a[12:15], v[136:139]\n"
"buffer_load_dwordx4 v19, s[44:47], s65 offen offset:3072 lds\n"
"v_mfma_f32_16x16x32_bf16 v[140:143], v[100:103], a[12:15], v[140:143]\n"
"s_waitcnt lgkmcnt(7)\n"
"v_mfma_f32_16x16x32_bf16 v[104:107], v[64:67], a[16:19], v[104:107]\n"
"ds_read_b128 v[84:87], v51 offset:15360\n"
"s_waitcnt lgkmcnt(7)\n"
"v_mfma_f32_16x16x32_bf16 v[108:111], v[68:71], a[16:19], v[108:111]\n"
"ds_read_b128 v[88:91], v51 offset:15616\n"
"s_add_u32 m0, s64, 12288\n"
"s_waitcnt lgkmcnt(7)\n"
"v_mfma_f32_16x16x32_bf16 v[112:115], v[72:75], a[16:19], v[112:115]\n"
"ds_read_b128 v[92:95], v51 offset:15872\n"
"buffer_load_dwordx4 v16, s[48:51], s65 offen lds\n"
"s_waitcnt lgkmcnt(7)\n"
"v_mfma_f32_16x16x32_bf16 v[116:119], v[76:79], a[16:19], v[116:119]\n"
"ds_read_b128 v[96:99], v51 offset:16128\n"
"s_waitcnt lgkmcnt(7)\n"
"v_mfma_f32_16x16x32_bf16 v[120:123], v[80:83], a[16:19], v[120:123]\n"
"ds_read_b128 v[100:103], v51 offset:16384\n"
"v_mfma_f32_16x16x32_bf16 v[124:127], v[64:67], a[20:23], v[124:127]\n"
"ds_read_b128 a[120:123], v28 offset:16384\n"
"s_add_u32 m0, s64, 12288\n"
"v_mfma_f32_16x16x32_bf16 v[128:131], v[68:71], a[20:23], v[128:131]\n"
"ds_read_b128 a[124:127], v28 offset:20480\n"
"buffer_load_dwordx4 v17, s[48:51], s65 offen offset:1024 lds\n"
"v_mfma_f32_16x16x32_bf16 v[132:135], v[72:75], a[20:23], v[132:135]\n"
"v_mfma_f32_16x16x32_bf16 v[136:139], v[76:79], a[20:23], v[136:139]\n"
"v_mfma_f32_16x16x32_bf16 v[140:143], v[80:83], a[20:23], v[140:143]\n"
"s_waitcnt lgkmcnt(6)\n"
"v_mfma_f32_16x16x32_bf16 v[104:107], v[84:87], a[24:27], v[104:107]\n"
"s_waitcnt lgkmcnt(5)\n"
"v_mfma_f32_16x16x32_bf16 v[108:111], v[88:91], a[24:27], v[108:111]\n"
"s_waitcnt lgkmcnt(4)\n"
"v_mfma_f32_16x16x32_bf16 v[112:115], v[92:95], a[24:27], v[112:115]\n"
"s_waitcnt lgkmcnt(0)\n"
"s_waitcnt vmcnt(15)\n"
"s_barrier\n"
"v_mfma_f32_16x16x32_bf16 v[116:119], v[96:99], a[24:27], v[116:119]\n"
"ds_read_b128 v[64:67], v29\n"
"v_mfma_f32_16x16x32_bf16 v[120:123], v[100:103], a[24:27], v[120:123]\n"
"ds_read_b128 v[68:71], v29 offset:256\n"
"v_mfma_f32_16x16x32_bf16 v[124:127], v[84:87], a[28:31], v[124:127]\n"
"ds_read_b128 v[72:75], v29 offset:512\n"
"v_mfma_f32_16x16x32_bf16 v[128:131], v[88:91], a[28:31], v[128:131]\n"
"ds_read_b128 v[76:79], v29 offset:768\n"
"v_mfma_f32_16x16x32_bf16 v[132:135], v[92:95], a[28:31], v[132:135]\n"
"ds_read_b128 v[80:83], v29 offset:1024\n"
"s_add_u32 m0, s37, 40960\n"
"v_mfma_f32_16x16x32_bf16 v[136:139], v[96:99], a[28:31], v[136:139]\n"
"buffer_load_dwordx4 v20, s[40:43], s67 offen sc1 lds\n"
"v_mfma_f32_16x16x32_bf16 v[140:143], v[100:103], a[28:31], v[140:143]\n"
"buffer_load_dwordx4 v20, s[40:43], s67 offen offset:1024 sc1 lds\n"
"s_waitcnt lgkmcnt(4)\n"
"v_mfma_f32_16x16x32_bf16 v[104:107], v[64:67], a[32:35], v[104:107]\n"
"ds_read_b128 v[84:87], v29 offset:5120\n"
"s_waitcnt lgkmcnt(4)\n"
"v_mfma_f32_16x16x32_bf16 v[108:111], v[68:71], a[32:35], v[108:111]\n"
"ds_read_b128 v[88:91], v29 offset:5376\n"
"s_waitcnt lgkmcnt(4)\n"
"v_mfma_f32_16x16x32_bf16 v[112:115], v[72:75], a[32:35], v[112:115]\n"
"ds_read_b128 v[92:95], v29 offset:5632\n"
"s_waitcnt lgkmcnt(4)\n"
"v_mfma_f32_16x16x32_bf16 v[116:119], v[76:79], a[32:35], v[116:119]\n"
"ds_read_b128 v[96:99], v29 offset:5888\n"
"s_waitcnt lgkmcnt(4)\n"
"v_mfma_f32_16x16x32_bf16 v[120:123], v[80:83], a[32:35], v[120:123]\n"
"ds_read_b128 v[100:103], v29 offset:6144\n"
"v_mfma_f32_16x16x32_bf16 v[124:127], v[64:67], a[36:39], v[124:127]\n"
"buffer_load_dwordx4 v20, s[40:43], s67 offen offset:2048 sc1 lds\n"
"v_mfma_f32_16x16x32_bf16 v[128:131], v[68:71], a[36:39], v[128:131]\n"
"buffer_load_dwordx4 v20, s[40:43], s67 offen offset:3072 sc1 lds\n"
"s_add_u32 m0, s37, 45056\n"
"v_mfma_f32_16x16x32_bf16 v[132:135], v[72:75], a[36:39], v[132:135]\n"
"buffer_load_dwordx4 v21, s[40:43], s67 offen sc1 lds\n"
"s_add_u32 s67, s67, 20480\n"
"s_cmp_eq_u32 s67, s69\n"
"s_cselect_b32 s67, s68, s67\n"
"s_add_u32 m0, s64, 12288\n"
"v_mfma_f32_16x16x32_bf16 v[136:139], v[76:79], a[36:39], v[136:139]\n"
"buffer_load_dwordx4 v18, s[48:51], s65 offen offset:2048 lds\n"
"s_add_u32 m0, s64, 12288\n"
"v_mfma_f32_16x16x32_bf16 v[140:143], v[80:83], a[36:39], v[140:143]\n"
"buffer_load_dwordx4 v19, s[48:51], s65 offen offset:3072 lds\n"
"s_waitcnt lgkmcnt(4)\n"
"v_mfma_f32_16x16x32_bf16 v[104:107], v[84:87], a[40:43], v[104:107]\n"
"ds_read_b128 v[64:67], v29 offset:10240\n"
"s_waitcnt lgkmcnt(4)\n"
"v_mfma_f32_16x16x32_bf16 v[108:111], v[88:91], a[40:43], v[108:111]\n"
"ds_read_b128 v[68:71], v29 offset:10496\n"
"s_waitcnt lgkmcnt(4)\n"
"v_mfma_f32_16x16x32_bf16 v[112:115], v[92:95], a[40:43], v[112:115]\n"
"ds_read_b128 v[72:75], v29 offset:10752\n"
"s_waitcnt lgkmcnt(4)\n"
"v_mfma_f32_16x16x32_bf16 v[116:119], v[96:99], a[40:43], v[116:119]\n"
"ds_read_b128 v[76:79], v29 offset:11008\n"
"s_waitcnt lgkmcnt(4)\n"
"v_mfma_f32_16x16x32_bf16 v[120:123], v[100:103], a[40:43], v[120:123]\n"
"ds_read_b128 v[80:83], v29 offset:11264\n"
"s_add_u32 s65, s65, 256\n"
"s_and_b32 s65, s65, 0xfff\n"
"s_add_u32 m0, s64, 16384\n"
"v_mfma_f32_16x16x32_bf16 v[124:127], v[84:87], a[44:47], v[124:127]\n"
"buffer_load_dwordx4 v16, s[44:47], s65 offen lds\n"
"v_mfma_f32_16x16x32_bf16 v[128:131], v[88:91], a[44:47], v[128:131]\n"
"s_add_u32 m0, s64, 16384\n"
"v_mfma_f32_16x16x32_bf16 v[132:135], v[92:95], a[44:47], v[132:135]\n"
"buffer_load_dwordx4 v17, s[44:47], s65 offen offset:1024 lds\n"
"s_add_u32 s74, s80, 0\n"
"v_mfma_f32_16x16x32_bf16 v[136:139], v[96:99], a[44:47], v[136:139]\n"
"buffer_load_dwordx4 a[0:3], v30, s[40:43], s74 offen sc1\n"
"s_add_u32 s74, s80, 0\n"
"v_mfma_f32_16x16x32_bf16 v[140:143], v[100:103], a[44:47], v[140:143]\n"
"buffer_load_dwordx4 a[4:7], v30, s[40:43], s74 offen offset:1024 sc1\n"
"s_waitcnt lgkmcnt(4)\n"
"v_mfma_f32_16x16x32_bf16 v[104:107], v[64:67], a[48:51], v[104:107]\n"
"ds_read_b128 v[84:87], v29 offset:15360\n"
"s_waitcnt lgkmcnt(4)\n"
"v_mfma_f32_16x16x32_bf16 v[108:111], v[68:71], a[48:51], v[108:111]\n"
"ds_read_b128 v[88:91], v29 offset:15616\n"
"s_waitcnt lgkmcnt(4)\n"
"v_mfma_f32_16x16x32_bf16 v[112:115], v[72:75], a[48:51], v[112:115]\n"
"ds_read_b128 v[92:95], v29 offset:15872\n"
"s_waitcnt lgkmcnt(4)\n"
"v_mfma_f32_16x16x32_bf16 v[116:119], v[76:79], a[48:51], v[116:119]\n"
"ds_read_b128 v[96:99], v29 offset:16128\n"
"s_waitcnt lgkmcnt(4)\n"
"v_mfma_f32_16x16x32_bf16 v[120:123], v[80:83], a[48:51], v[120:123]\n"
"ds_read_b128 v[100:103], v29 offset:16384\n"
"s_add_u32 s74, s80, 0\n"
"v_mfma_f32_16x16x32_bf16 v[124:127], v[64:67], a[52:55], v[124:127]\n"
"buffer_load_dwordx4 a[8:11], v30, s[40:43], s74 offen offset:2048 sc1\n"
"s_add_u32 s74, s80, 0\n"
"v_mfma_f32_16x16x32_bf16 v[128:131], v[68:71], a[52:55], v[128:131]\n"
"buffer_load_dwordx4 a[12:15], v30, s[40:43], s74 offen offset:3072 sc1\n"
"s_add_u32 s74, s80, 4096\n"
"v_mfma_f32_16x16x32_bf16 v[132:135], v[72:75], a[52:55], v[132:135]\n"
"buffer_load_dwordx4 a[16:19], v30, s[40:43], s74 offen sc1\n"
"s_add_u32 s74, s80, 4096\n"
"v_mfma_f32_16x16x32_bf16 v[136:139], v[76:79], a[52:55], v[136:139]\n"
"buffer_load_dwordx4 a[20:23], v30, s[40:43], s74 offen offset:1024 sc1\n"
"v_mfma_f32_16x16x32_bf16 v[140:143], v[80:83], a[52:55], v[140:143]\n"
"s_waitcnt lgkmcnt(4)\n"
"v_mfma_f32_16x16x32_bf16 v[104:107], v[84:87], a[56:59], v[104:107]\n"
"s_waitcnt lgkmcnt(3)\n"
"v_mfma_f32_16x16x32_bf16 v[108:111], v[88:91], a[56:59], v[108:111]\n"
"s_waitcnt lgkmcnt(2)\n"
"v_mfma_f32_16x16x32_bf16 v[112:115], v[92:95], a[56:59], v[112:115]\n"
"s_waitcnt lgkmcnt(0)\n"
"s_waitcnt vmcnt(20)\n"
"s_barrier\n"
"v_mfma_f32_16x16x32_bf16 v[116:119], v[96:99], a[56:59], v[116:119]\n"
"ds_read_b128 v[64:67], v29 offset:20480\n"
"s_add_u32 m0, s37, 0\n"
"s_nop 0\n"
"buffer_load_dwordx4 v20, s[40:43], s67 offen sc1 lds\n"
"v_mfma_f32_16x16x32_bf16 v[120:123], v[100:103], a[56:59], v[120:123]\n"
"ds_read_b128 v[68:71], v29 offset:20736\n"
"v_mfma_f32_16x16x32_bf16 v[124:127], v[84:87], a[60:63], v[124:127]\n"
"ds_read_b128 v[72:75], v29 offset:20992\n"
"buffer_load_dwordx4 v20, s[40:43], s67 offen offset:1024 sc1 lds\n"
"v_mfma_f32_16x16x32_bf16 v[128:131], v[88:91], a[60:63], v[128:131]\n"
"ds_read_b128 v[76:79], v29 offset:21248\n"
"v_mfma_f32_16x16x32_bf16 v[132:135], v[92:95], a[60:63], v[132:135]\n"
"ds_read_b128 v[80:83], v29 offset:21504\n"
"buffer_load_dwordx4 v20, s[40:43], s67 offen offset:2048 sc1 lds\n"
"v_mfma_f32_16x16x32_bf16 v[136:139], v[96:99], a[60:63], v[136:139]\n"
"v_mfma_f32_16x16x32_bf16 v[140:143], v[100:103], a[60:63], v[140:143]\n"
"buffer_load_dwordx4 v20, s[40:43], s67 offen offset:3072 sc1 lds\n"
"s_waitcnt lgkmcnt(4)\n"
"v_mfma_f32_16x16x32_bf16 v[104:107], v[64:67], a[64:67], v[104:107]\n"
"ds_read_b128 v[84:87], v29 offset:25600\n"
"s_waitcnt lgkmcnt(4)\n"
"v_mfma_f32_16x16x32_bf16 v[108:111], v[68:71], a[64:67], v[108:111]\n"
"ds_read_b128 v[88:91], v29 offset:25856\n"
"s_add_u32 m0, s37, 4096\n"
"s_waitcnt lgkmcnt(4)\n"
"v_mfma_f32_16x16x32_bf16 v[112:115], v[72:75], a[64:67], v[112:115]\n"
"ds_read_b128 v[92:95], v29 offset:26112\n"
"buffer_load_dwordx4 v21, s[40:43], s67 offen sc1 lds\n"
"s_waitcnt lgkmcnt(4)\n"
"v_mfma_f32_16x16x32_bf16 v[116:119], v[76:79], a[64:67], v[116:119]\n"
"ds_read_b128 v[96:99], v29 offset:26368\n"
"s_add_u32 s67, s67, 20480\n"
"s_cmp_eq_u32 s67, s69\n"
"s_cselect_b32 s67, s68, s67\n"
"s_add_u32 m0, s64, 16384\n"
"s_waitcnt lgkmcnt(4)\n"
"v_mfma_f32_16x16x32_bf16 v[120:123], v[80:83], a[64:67], v[120:123]\n"
"ds_read_b128 v[100:103], v29 offset:26624\n"
"buffer_load_dwordx4 v18, s[44:47], s65 offen offset:2048 lds\n"
"v_mfma_f32_16x16x32_bf16 v[124:127], v[64:67], a[68:71], v[124:127]\n"
"ds_read_b128 a[128:131], v25\n"
"s_add_u32 m0, s64, 16384\n"
"v_mfma_f32_16x16x32_bf16 v[128:131], v[68:71], a[68:71], v[128:131]\n"
"ds_read_b128 a[132:135], v25 offset:4096\n"
"buffer_load_dwordx4 v19, s[44:47], s65 offen offset:3072 lds\n"
"v_mfma_f32_16x16x32_bf16 v[132:135], v[72:75], a[68:71], v[132:135]\n"
"ds_read_b128 a[136:139], v26\n"
"v_mfma_f32_16x16x32_bf16 v[136:139], v[76:79], a[68:71], v[136:139]\n"
"s_add_u32 m0, s64, 20480\n"
"v_mfma_f32_16x16x32_bf16 v[140:143], v[80:83], a[68:71], v[140:143]\n"
"buffer_load_dwordx4 v16, s[48:51], s65 offen lds\n"
"s_waitcnt lgkmcnt(7)\n"
"v_mfma_f32_16x16x32_bf16 v[104:107], v[84:87], a[72:75], v[104:107]\n"
"ds_read_b128 v[64:67], v29 offset:30720\n"
"s_add_u32 m0, s64, 20480\n"
"s_waitcnt lgkmcnt(7)\n"
"v_mfma_f32_16x16x32_bf16 v[108:111], v[88:91], a[72:75], v[108:111]\n"
"ds_read_b128 v[68:71], v29 offset:30976\n"
"buffer_load_dwordx4 v17, s[48:51], s65 offen offset:1024 lds\n"
"s_waitcnt lgkmcnt(7)\n"
"v_mfma_f32_16x16x32_bf16 v[112:115], v[92:95], a[72:75], v[112:115]\n"
"ds_read_b128 v[72:75], v29 offset:31232\n"
"s_add_u32 m0, s64, 20480\n"
"s_waitcnt lgkmcnt(7)\n"
"v_mfma_f32_16x16x32_bf16 v[116:119], v[96:99], a[72:75], v[116:119]\n"
"ds_read_b128 v[76:79], v29 offset:31488\n"
"buffer_load_dwordx4 v18, s[48:51], s65 offen offset:2048 lds\n"
"s_waitcnt lgkmcnt(7)\n"
"v_mfma_f32_16x16x32_bf16 v[120:123], v[100:103], a[72:75], v[120:123]\n"
"ds_read_b128 v[80:83], v29 offset:31744\n"
"v_mfma_f32_16x16x32_bf16 v[124:127], v[84:87], a[76:79], v[124:127]\n"
"ds_read_b128 a[140:143], v26 offset:4096\n"
"s_add_u32 s74, s80, 4096\n"
"v_mfma_f32_16x16x32_bf16 v[128:131], v[88:91], a[76:79], v[128:131]\n"
"ds_read_b128 a[144:147], v27\n"
"buffer_load_dwordx4 a[24:27], v30, s[40:43], s74 offen offset:2048 sc1\n"
"v_mfma_f32_16x16x32_bf16 v[132:135], v[92:95], a[76:79], v[132:135]\n"
"ds_read_b128 a[148:151], v27 offset:4096\n"
"s_add_u32 s74, s80, 4096\n"
"v_mfma_f32_16x16x32_bf16 v[136:139], v[96:99], a[76:79], v[136:139]\n"
"buffer_load_dwordx4 a[28:31], v30, s[40:43], s74 offen offset:3072 sc1\n"
"v_mfma_f32_16x16x32_bf16 v[140:143], v[100:103], a[76:79], v[140:143]\n"
"s_add_u32 s74, s80, 8192\n"
"s_waitcnt lgkmcnt(7)\n"
"v_mfma_f32_16x16x32_bf16 v[104:107], v[64:67], a[80:83], v[104:107]\n"
"ds_read_b128 v[84:87], v29 offset:35840\n"
"buffer_load_dwordx4 a[32:35], v30, s[40:43], s74 offen sc1\n"
"s_waitcnt lgkmcnt(7)\n"
"v_mfma_f32_16x16x32_bf16 v[108:111], v[68:71], a[80:83], v[108:111]\n"
"ds_read_b128 v[88:91], v29 offset:36096\n"
"s_waitcnt lgkmcnt(7)\n"
"v_mfma_f32_16x16x32_bf16 v[112:115], v[72:75], a[80:83], v[112:115]\n"
"ds_read_b128 v[92:95], v29 offset:36352\n"
"s_add_u32 s74, s80, 8192\n"
"s_waitcnt lgkmcnt(7)\n"
"v_mfma_f32_16x16x32_bf16 v[116:119], v[76:79], a[80:83], v[116:119]\n"
"ds_read_b128 v[96:99], v29 offset:36608\n"
"buffer_load_dwordx4 a[36:39], v30, s[40:43], s74 offen offset:1024 sc1\n"
"s_waitcnt lgkmcnt(7)\n"
"v_mfma_f32_16x16x32_bf16 v[120:123], v[80:83], a[80:83], v[120:123]\n"
"ds_read_b128 v[100:103], v29 offset:36864\n"
"s_add_u32 s74, s80, 8192\n"
"v_mfma_f32_16x16x32_bf16 v[124:127], v[64:67], a[84:87], v[124:127]\n"
"ds_read_b128 a[152:155], v28\n"
"buffer_load_dwordx4 a[40:43], v30, s[40:43], s74 offen offset:2048 sc1\n"
"v_mfma_f32_16x16x32_bf16 v[128:131], v[68:71], a[84:87], v[128:131]\n"
"ds_read_b128 a[156:159], v28 offset:4096\n"
"s_add_u32 s74, s80, 8192\n"
"v_mfma_f32_16x16x32_bf16 v[132:135], v[72:75], a[84:87], v[132:135]\n"
"buffer_load_dwordx4 a[44:47], v30, s[40:43], s74 offen offset:3072 sc1\n"
"v_mfma_f32_16x16x32_bf16 v[136:139], v[76:79], a[84:87], v[136:139]\n"
"v_mfma_f32_16x16x32_bf16 v[140:143], v[80:83], a[84:87], v[140:143]\n"
"s_waitcnt lgkmcnt(6)\n"
"v_mfma_f32_16x16x32_bf16 v[104:107], v[84:87], a[88:91], v[104:107]\n"
"s_waitcnt lgkmcnt(5)\n"
"v_mfma_f32_16x16x32_bf16 v[108:111], v[88:91], a[88:91], v[108:111]\n"
"s_waitcnt lgkmcnt(4)\n"
"v_mfma_f32_16x16x32_bf16 v[112:115], v[92:95], a[88:91], v[112:115]\n"
"s_waitcnt lgkmcnt(0)\n"
"s_waitcnt vmcnt(26)\n"
"s_barrier\n"
"v_mfma_f32_16x16x32_bf16 v[116:119], v[96:99], a[88:91], v[116:119]\n"
"ds_read_b128 v[64:67], v51\n"
"s_add_u32 m0, s37, 20480\n"
"s_nop 0\n"
"buffer_load_dwordx4 v20, s[40:43], s67 offen sc1 lds\n"
"v_mfma_f32_16x16x32_bf16 v[120:123], v[100:103], a[88:91], v[120:123]\n"
"ds_read_b128 v[68:71], v51 offset:256\n"
"v_mfma_f32_16x16x32_bf16 v[124:127], v[84:87], a[92:95], v[124:127]\n"
"ds_read_b128 v[72:75], v51 offset:512\n"
"buffer_load_dwordx4 v20, s[40:43], s67 offen offset:1024 sc1 lds\n"
"v_mfma_f32_16x16x32_bf16 v[128:131], v[88:91], a[92:95], v[128:131]\n"
"ds_read_b128 v[76:79], v51 offset:768\n"
"v_mfma_f32_16x16x32_bf16 v[132:135], v[92:95], a[92:95], v[132:135]\n"
"ds_read_b128 v[80:83], v51 offset:1024\n"
"buffer_load_dwordx4 v20, s[40:43], s67 offen offset:2048 sc1 lds\n"
"v_mfma_f32_16x16x32_bf16 v[136:139], v[96:99], a[92:95], v[136:139]\n"
"v_mfma_f32_16x16x32_bf16 v[140:143], v[100:103], a[92:95], v[140:143]\n"
"buffer_load_dwordx4 v20, s[40:43], s67 offen offset:3072 sc1 lds\n"
"s_waitcnt vmcnt(28)\n"
"s_waitcnt lgkmcnt(4)\n"
"v_mfma_f32_16x16x32_bf16 v[104:107], v[64:67], a[96:99], v[104:107]\n"
"ds_read_b128 v[84:87], v51 offset:5120\n"
"s_waitcnt lgkmcnt(4)\n"
"v_mfma_f32_16x16x32_bf16 v[108:111], v[68:71], a[96:99], v[108:111]\n"
"ds_read_b128 v[88:91], v51 offset:5376\n"
"s_add_u32 m0, s37, 24576\n"
"s_waitcnt lgkmcnt(4)\n"
"v_mfma_f32_16x16x32_bf16 v[112:115], v[72:75], a[96:99], v[112:115]\n"
"ds_read_b128 v[92:95], v51 offset:5632\n"
"buffer_load_dwordx4 v21, s[40:43], s67 offen sc1 lds\n"
"s_waitcnt lgkmcnt(4)\n"
"v_mfma_f32_16x16x32_bf16 v[116:119], v[76:79], a[96:99], v[116:119]\n"
"ds_read_b128 v[96:99], v51 offset:5888\n"
"s_add_u32 s67, s67, 20480\n"
"s_cmp_eq_u32 s67, s69\n"
"s_cselect_b32 s67, s68, s67\n"
"s_add_u32 m0, s64, 20480\n"
"s_waitcnt lgkmcnt(4)\n"
"v_mfma_f32_16x16x32_bf16 v[120:123], v[80:83], a[96:99], v[120:123]\n"
"ds_read_b128 v[100:103], v51 offset:6144\n"
"buffer_load_dwordx4 v19, s[48:51], s65 offen offset:3072 lds\n"
"v_mfma_f32_16x16x32_bf16 v[124:127], v[64:67], a[100:103], v[124:127]\n"
"ds_read_b128 a[160:163], v25 offset:8192\n"
"s_add_u32 s65, s65, 256\n"
"s_and_b32 s65, s65, 0xfff\n"
"s_add_u32 m0, s64, 0\n"
"v_mfma_f32_16x16x32_bf16 v[128:131], v[68:71], a[100:103], v[128:131]\n"
"ds_read_b128 a[164:167], v25 offset:12288\n"
"buffer_load_dwordx4 v16, s[44:47], s65 offen lds\n"
"v_mfma_f32_16x16x32_bf16 v[132:135], v[72:75], a[100:103], v[132:135]\n"
"ds_read_b128 a[168:171], v26 offset:8192\n"
"v_mfma_f32_16x16x32_bf16 v[136:139], v[76:79], a[100:103], v[136:139]\n"
"s_add_u32 m0, s64, 0\n"
"v_mfma_f32_16x16x32_bf16 v[140:143], v[80:83], a[100:103], v[140:143]\n"
"buffer_load_dwordx4 v17, s[44:47], s65 offen offset:1024 lds\n"
"s_waitcnt lgkmcnt(7)\n"
"v_mfma_f32_16x16x32_bf16 v[104:107], v[84:87], a[104:107], v[104:107]\n"
"ds_read_b128 v[64:67], v51 offset:10240\n"
"s_add_u32 m0, s64, 0\n"
"s_waitcnt lgkmcnt(7)\n"
"v_mfma_f32_16x16x32_bf16 v[108:111], v[88:91], a[104:107], v[108:111]\n"
"ds_read_b128 v[68:71], v51 offset:10496\n"
"buffer_load_dwordx4 v18, s[44:47], s65 offen offset:2048 lds\n"
"s_waitcnt lgkmcnt(7)\n"
"v_mfma_f32_16x16x32_bf16 v[112:115], v[92:95], a[104:107], v[112:115]\n"
"ds_read_b128 v[72:75], v51 offset:10752\n"
"s_add_u32 m0, s64, 0\n"
"s_waitcnt lgkmcnt(7)\n"
"v_mfma_f32_16x16x32_bf16 v[116:119], v[96:99], a[104:107], v[116:119]\n"
"ds_read_b128 v[76:79], v51 offset:11008\n"
"buffer_load_dwordx4 v19, s[44:47], s65 offen offset:3072 lds\n"
"s_waitcnt lgkmcnt(7)\n"
"v_mfma_f32_16x16x32_bf16 v[120:123], v[100:103], a[104:107], v[120:123]\n"
"ds_read_b128 v[80:83], v51 offset:11264\n"
"v_mfma_f32_16x16x32_bf16 v[124:127], v[84:87], a[108:111], v[124:127]\n"
"ds_read_b128 a[172:175], v26 offset:12288\n"
"s_add_u32 s74, s80, 12288\n"
"v_mfma_f32_16x16x32_bf16 v[128:131], v[88:91], a[108:111], v[128:131]\n"
"ds_read_b128 a[176:179], v27 offset:8192\n"
"buffer_load_dwordx4 a[48:51], v30, s[40:43], s74 offen sc1\n"
"v_mfma_f32_16x16x32_bf16 v[132:135], v[92:95], a[108:111], v[132:135]\n"
"ds_read_b128 a[180:183], v27 offset:12288\n"
"s_add_u32 s74, s80, 12288\n"
"v_mfma_f32_16x16x32_bf16 v[136:139], v[96:99], a[108:111], v[136:139]\n"
"buffer_load_dwordx4 a[52:55], v30, s[40:43], s74 offen offset:1024 sc1\n"
"v_mfma_f32_16x16x32_bf16 v[140:143], v[100:103], a[108:111], v[140:143]\n"
"s_add_u32 s74, s80, 12288\n"
"s_waitcnt lgkmcnt(7)\n"
"v_mfma_f32_16x16x32_bf16 v[104:107], v[64:67], a[112:115], v[104:107]\n"
"ds_read_b128 v[84:87], v51 offset:15360\n"
"buffer_load_dwordx4 a[56:59], v30, s[40:43], s74 offen offset:2048 sc1\n"
"s_waitcnt lgkmcnt(7)\n"
"v_mfma_f32_16x16x32_bf16 v[108:111], v[68:71], a[112:115], v[108:111]\n"
"ds_read_b128 v[88:91], v51 offset:15616\n"
"s_waitcnt lgkmcnt(7)\n"
"v_mfma_f32_16x16x32_bf16 v[112:115], v[72:75], a[112:115], v[112:115]\n"
"ds_read_b128 v[92:95], v51 offset:15872\n"
"s_add_u32 s74, s80, 12288\n"
"s_waitcnt lgkmcnt(7)\n"
"v_mfma_f32_16x16x32_bf16 v[116:119], v[76:79], a[112:115], v[116:119]\n"
"ds_read_b128 v[96:99], v51 offset:16128\n"
"buffer_load_dwordx4 a[60:63], v30, s[40:43], s74 offen offset:3072 sc1\n"
"s_waitcnt lgkmcnt(7)\n"
"v_mfma_f32_16x16x32_bf16 v[120:123], v[80:83], a[112:115], v[120:123]\n"
"ds_read_b128 v[100:103], v51 offset:16384\n"
"s_add_u32 s74, s80, 16384\n"
"v_mfma_f32_16x16x32_bf16 v[124:127], v[64:67], a[116:119], v[124:127]\n"
"ds_read_b128 a[184:187], v28 offset:8192\n"
"buffer_load_dwordx4 a[64:67], v30, s[40:43], s74 offen sc1\n"
"v_mfma_f32_16x16x32_bf16 v[128:131], v[68:71], a[116:119], v[128:131]\n"
"ds_read_b128 a[188:191], v28 offset:12288\n"
"s_add_u32 s74, s80, 16384\n"
"v_mfma_f32_16x16x32_bf16 v[132:135], v[72:75], a[116:119], v[132:135]\n"
"buffer_load_dwordx4 a[68:71], v30, s[40:43], s74 offen offset:1024 sc1\n"
"v_mfma_f32_16x16x32_bf16 v[136:139], v[76:79], a[116:119], v[136:139]\n"
"v_mfma_f32_16x16x32_bf16 v[140:143], v[80:83], a[116:119], v[140:143]\n"
"s_waitcnt lgkmcnt(6)\n"
"v_mfma_f32_16x16x32_bf16 v[104:107], v[84:87], a[120:123], v[104:107]\n"
"s_waitcnt lgkmcnt(5)\n"
"v_mfma_f32_16x16x32_bf16 v[108:111], v[88:91], a[120:123], v[108:111]\n"
"s_waitcnt lgkmcnt(4)\n"
"v_mfma_f32_16x16x32_bf16 v[112:115], v[92:95], a[120:123], v[112:115]\n"
"s_waitcnt lgkmcnt(0)\n"
"s_waitcnt vmcnt(27)\n"
"s_barrier\n"
"v_mfma_f32_16x16x32_bf16 v[116:119], v[96:99], a[120:123], v[116:119]\n"
"ds_read_b128 v[64:67], v29\n"
"v_mfma_f32_16x16x32_bf16 v[120:123], v[100:103], a[120:123], v[120:123]\n"
"ds_read_b128 v[68:71], v29 offset:256\n"
"v_mfma_f32_16x16x32_bf16 v[124:127], v[84:87], a[124:127], v[124:127]\n"
"ds_read_b128 v[72:75], v29 offset:512\n"
"v_mfma_f32_16x16x32_bf16 v[128:131], v[88:91], a[124:127], v[128:131]\n"
"ds_read_b128 v[76:79], v29 offset:768\n"
"v_mfma_f32_16x16x32_bf16 v[132:135], v[92:95], a[124:127], v[132:135]\n"
"ds_read_b128 v[80:83], v29 offset:1024\n"
"s_add_u32 m0, s37, 40960\n"
"v_mfma_f32_16x16x32_bf16 v[136:139], v[96:99], a[124:127], v[136:139]\n"
"buffer_load_dwordx4 v20, s[40:43], s67 offen sc1 lds\n"
"v_mfma_f32_16x16x32_bf16 v[140:143], v[100:103], a[124:127], v[140:143]\n"
"buffer_load_dwordx4 v20, s[40:43], s67 offen offset:1024 sc1 lds\n"
"s_waitcnt lgkmcnt(4)\n"
"v_mfma_f32_16x16x32_bf16 v[104:107], v[64:67], a[128:131], v[104:107]\n"
"ds_read_b128 v[84:87], v29 offset:5120\n"
"s_waitcnt lgkmcnt(4)\n"
"v_mfma_f32_16x16x32_bf16 v[108:111], v[68:71], a[128:131], v[108:111]\n"
"ds_read_b128 v[88:91], v29 offset:5376\n"
"s_waitcnt lgkmcnt(4)\n"
"v_mfma_f32_16x16x32_bf16 v[112:115], v[72:75], a[128:131], v[112:115]\n"
"ds_read_b128 v[92:95], v29 offset:5632\n"
"s_waitcnt lgkmcnt(4)\n"
"v_mfma_f32_16x16x32_bf16 v[116:119], v[76:79], a[128:131], v[116:119]\n"
"ds_read_b128 v[96:99], v29 offset:5888\n"
"s_waitcnt lgkmcnt(4)\n"
"v_mfma_f32_16x16x32_bf16 v[120:123], v[80:83], a[128:131], v[120:123]\n"
"ds_read_b128 v[100:103], v29 offset:6144\n"
"v_mfma_f32_16x16x32_bf16 v[124:127], v[64:67], a[132:135], v[124:127]\n"
"buffer_load_dwordx4 v20, s[40:43], s67 offen offset:2048 sc1 lds\n"
"v_mfma_f32_16x16x32_bf16 v[128:131], v[68:71], a[132:135], v[128:131]\n"
"buffer_load_dwordx4 v20, s[40:43], s67 offen offset:3072 sc1 lds\n"
"s_add_u32 m0, s37, 45056\n"
"v_mfma_f32_16x16x32_bf16 v[132:135], v[72:75], a[132:135], v[132:135]\n"
"buffer_load_dwordx4 v21, s[40:43], s67 offen sc1 lds\n"
"v_mfma_f32_16x16x32_bf16 v[136:139], v[76:79], a[132:135], v[136:139]\n"
"s_add_u32 s67, s67, 20480\n"
"s_cmp_eq_u32 s67, s69\n"
"s_cselect_b32 s67, s68, s67\n"
"s_add_u32 m0, s64, 4096\n"
"v_mfma_f32_16x16x32_bf16 v[140:143], v[80:83], a[132:135], v[140:143]\n"
"buffer_load_dwordx4 v16, s[48:51], s65 offen lds\n"
"s_waitcnt lgkmcnt(4)\n"
"v_mfma_f32_16x16x32_bf16 v[104:107], v[84:87], a[136:139], v[104:107]\n"
"ds_read_b128 v[64:67], v29 offset:10240\n"
"s_waitcnt lgkmcnt(4)\n"
"v_mfma_f32_16x16x32_bf16 v[108:111], v[88:91], a[136:139], v[108:111]\n"
"ds_read_b128 v[68:71], v29 offset:10496\n"
"s_waitcnt lgkmcnt(4)\n"
"v_mfma_f32_16x16x32_bf16 v[112:115], v[92:95], a[136:139], v[112:115]\n"
"ds_read_b128 v[72:75], v29 offset:10752\n"
"s_waitcnt lgkmcnt(4)\n"
"v_mfma_f32_16x16x32_bf16 v[116:119], v[96:99], a[136:139], v[116:119]\n"
"ds_read_b128 v[76:79], v29 offset:11008\n"
"s_waitcnt lgkmcnt(4)\n"
"v_mfma_f32_16x16x32_bf16 v[120:123], v[100:103], a[136:139], v[120:123]\n"
"ds_read_b128 v[80:83], v29 offset:11264\n"
"s_add_u32 m0, s64, 4096\n"
"v_mfma_f32_16x16x32_bf16 v[124:127], v[84:87], a[140:143], v[124:127]\n"
"buffer_load_dwordx4 v17, s[48:51], s65 offen offset:1024 lds\n"
"s_add_u32 m0, s64, 4096\n"
"v_mfma_f32_16x16x32_bf16 v[128:131], v[88:91], a[140:143], v[128:131]\n"
"buffer_load_dwordx4 v18, s[48:51], s65 offen offset:2048 lds\n"
"s_add_u32 m0, s64, 4096\n"
"v_mfma_f32_16x16x32_bf16 v[132:135], v[92:95], a[140:143], v[132:135]\n"
"buffer_load_dwordx4 v19, s[48:51], s65 offen offset:3072 lds\n"
"s_add_u32 s65, s65, 256\n"
"s_and_b32 s65, s65, 0xfff\n"
"s_add_u32 s74, s80, 16384\n"
"v_mfma_f32_16x16x32_bf16 v[136:139], v[96:99], a[140:143], v[136:139]\n"
"buffer_load_dwordx4 a[72:75], v30, s[40:43], s74 offen offset:2048 sc1\n"
"v_mfma_f32_16x16x32_bf16 v[140:143], v[100:103], a[140:143], v[140:143]\n"
"s_waitcnt lgkmcnt(4)\n"
"v_mfma_f32_16x16x32_bf16 v[104:107], v[64:67], a[144:147], v[104:107]\n"
"ds_read_b128 v[84:87], v29 offset:15360\n"
"s_waitcnt lgkmcnt(4)\n"
"v_mfma_f32_16x16x32_bf16 v[108:111], v[68:71], a[144:147], v[108:111]\n"
"ds_read_b128 v[88:91], v29 offset:15616\n"
"s_waitcnt lgkmcnt(4)\n"
"v_mfma_f32_16x16x32_bf16 v[112:115], v[72:75], a[144:147], v[112:115]\n"
"ds_read_b128 v[92:95], v29 offset:15872\n"
"s_waitcnt lgkmcnt(4)\n"
"v_mfma_f32_16x16x32_bf16 v[116:119], v[76:79], a[144:147], v[116:119]\n"
"ds_read_b128 v[96:99], v29 offset:16128\n"
"s_waitcnt lgkmcnt(4)\n"
"v_mfma_f32_16x16x32_bf16 v[120:123], v[80:83], a[144:147], v[120:123]\n"
"ds_read_b128 v[100:103], v29 offset:16384\n"
"s_add_u32 s74, s80, 16384\n"
"v_mfma_f32_16x16x32_bf16 v[124:127], v[64:67], a[148:151], v[124:127]\n"
"buffer_load_dwordx4 a[76:79], v30, s[40:43], s74 offen offset:3072 sc1\n"
"s_add_u32 s74, s80, 20480\n"
"v_mfma_f32_16x16x32_bf16 v[128:131], v[68:71], a[148:151], v[128:131]\n"
"buffer_load_dwordx4 a[80:83], v30, s[40:43], s74 offen sc1\n"
"s_add_u32 s74, s80, 20480\n"
"v_mfma_f32_16x16x32_bf16 v[132:135], v[72:75], a[148:151], v[132:135]\n"
"buffer_load_dwordx4 a[84:87], v30, s[40:43], s74 offen offset:1024 sc1\n"
"s_add_u32 s74, s80, 20480\n"
"v_mfma_f32_16x16x32_bf16 v[136:139], v[76:79], a[148:151], v[136:139]\n"
"buffer_load_dwordx4 a[88:91], v30, s[40:43], s74 offen offset:2048 sc1\n"
"v_mfma_f32_16x16x32_bf16 v[140:143], v[80:83], a[148:151], v[140:143]\n"
"s_waitcnt lgkmcnt(4)\n"
"v_mfma_f32_16x16x32_bf16 v[104:107], v[84:87], a[152:155], v[104:107]\n"
"s_waitcnt lgkmcnt(3)\n"
"v_mfma_f32_16x16x32_bf16 v[108:111], v[88:91], a[152:155], v[108:111]\n"
"s_waitcnt lgkmcnt(2)\n"
"v_mfma_f32_16x16x32_bf16 v[112:115], v[92:95], a[152:155], v[112:115]\n"
"s_waitcnt lgkmcnt(0)\n"
"s_waitcnt vmcnt(25)\n"
"s_barrier\n"
"v_mfma_f32_16x16x32_bf16 v[116:119], v[96:99], a[152:155], v[116:119]\n"
"ds_read_b128 v[64:67], v29 offset:20480\n"
"s_add_u32 m0, s37, 0\n"
"s_nop 0\n"
"buffer_load_dwordx4 v20, s[40:43], s67 offen sc1 lds\n"
"v_mfma_f32_16x16x32_bf16 v[120:123], v[100:103], a[152:155], v[120:123]\n"
"ds_read_b128 v[68:71], v29 offset:20736\n"
"v_mfma_f32_16x16x32_bf16 v[124:127], v[84:87], a[156:159], v[124:127]\n"
"ds_read_b128 v[72:75], v29 offset:20992\n"
"v_mfma_f32_16x16x32_bf16 v[128:131], v[88:91], a[156:159], v[128:131]\n"
"ds_read_b128 v[76:79], v29 offset:21248\n"
"buffer_load_dwordx4 v20, s[40:43], s67 offen offset:1024 sc1 lds\n"
"v_mfma_f32_16x16x32_bf16 v[132:135], v[92:95], a[156:159], v[132:135]\n"
"ds_read_b128 v[80:83], v29 offset:21504\n"
"v_mfma_f32_16x16x32_bf16 v[136:139], v[96:99], a[156:159], v[136:139]\n"
"v_mfma_f32_16x16x32_bf16 v[140:143], v[100:103], a[156:159], v[140:143]\n"
"s_waitcnt vmcnt(26)\n"
"s_waitcnt lgkmcnt(4)\n"
"v_mfma_f32_16x16x32_bf16 v[104:107], v[64:67], a[160:163], v[104:107]\n"
"ds_read_b128 v[84:87], v29 offset:25600\n"
"buffer_load_dwordx4 v20, s[40:43], s67 offen offset:2048 sc1 lds\n"
"s_waitcnt lgkmcnt(4)\n"
"v_mfma_f32_16x16x32_bf16 v[108:111], v[68:71], a[160:163], v[108:111]\n"
"ds_read_b128 v[88:91], v29 offset:25856\n"
"s_waitcnt lgkmcnt(4)\n"
"v_mfma_f32_16x16x32_bf16 v[112:115], v[72:75], a[160:163], v[112:115]\n"
"ds_read_b128 v[92:95], v29 offset:26112\n"
"s_waitcnt lgkmcnt(4)\n"
"v_mfma_f32_16x16x32_bf16 v[116:119], v[76:79], a[160:163], v[116:119]\n"
"ds_read_b128 v[96:99], v29 offset:26368\n"
"s_waitcnt lgkmcnt(4)\n"
"v_mfma_f32_16x16x32_bf16 v[120:123], v[80:83], a[160:163], v[120:123]\n"
"ds_read_b128 v[100:103], v29 offset:26624\n"
"buffer_load_dwordx4 v20, s[40:43], s67 offen offset:3072 sc1 lds\n"
"v_mfma_f32_16x16x32_bf16 v[124:127], v[64:67], a[164:167], v[124:127]\n"
"ds_read_b128 a[192:195], v25 offset:16384\n"
"v_mfma_f32_16x16x32_bf16 v[128:131], v[68:71], a[164:167], v[128:131]\n"
"ds_read_b128 a[196:199], v25 offset:20480\n"
"s_add_u32 m0, s37, 4096\n"
"v_mfma_f32_16x16x32_bf16 v[132:135], v[72:75], a[164:167], v[132:135]\n"
"ds_read_b128 a[200:203], v26 offset:16384\n"
"buffer_load_dwordx4 v21, s[40:43], s67 offen sc1 lds\n"
"v_mfma_f32_16x16x32_bf16 v[136:139], v[76:79], a[164:167], v[136:139]\n"
"v_mfma_f32_16x16x32_bf16 v[140:143], v[80:83], a[164:167], v[140:143]\n"
"s_waitcnt lgkmcnt(7)\n"
"v_mfma_f32_16x16x32_bf16 v[104:107], v[84:87], a[168:171], v[104:107]\n"
"ds_read_b128 v[64:67], v29 offset:30720\n"
"s_add_u32 s67, s67, 20480\n"
"s_cmp_eq_u32 s67, s69\n"
"s_cselect_b32 s67, s68, s67\n"
"s_add_u32 s74, s80, 20480\n"
"s_waitcnt lgkmcnt(7)\n"
"v_mfma_f32_16x16x32_bf16 v[108:111], v[88:91], a[168:171], v[108:111]\n"
"ds_read_b128 v[68:71], v29 offset:30976\n"
"buffer_load_dwordx4 a[92:95], v30, s[40:43], s74 offen offset:3072 sc1\n"
"s_waitcnt lgkmcnt(7)\n"
"v_mfma_f32_16x16x32_bf16 v[112:115], v[92:95], a[168:171], v[112:115]\n"
"ds_read_b128 v[72:75], v29 offset:31232\n"
"s_waitcnt lgkmcnt(7)\n"
"v_mfma_f32_16x16x32_bf16 v[116:119], v[96:99], a[168:171], v[116:119]\n"
"ds_read_b128 v[76:79], v29 offset:31488\n"
"s_waitcnt lgkmcnt(7)\n"
"v_mfma_f32_16x16x32_bf16 v[120:123], v[100:103], a[168:171], v[120:123]\n"
"ds_read_b128 v[80:83], v29 offset:31744\n"
"s_add_u32 s74, s80, 24576\n"
"v_mfma_f32_16x16x32_bf16 v[124:127], v[84:87], a[172:175], v[124:127]\n"
"ds_read_b128 a[204:207], v26 offset:20480\n"
"buffer_load_dwordx4 a[96:99], v30, s[40:43], s74 offen sc1\n"
"v_mfma_f32_16x16x32_bf16 v[128:131], v[88:91], a[172:175], v[128:131]\n"
"ds_read_b128 a[208:211], v27 offset:16384\n"
"v_mfma_f32_16x16x32_bf16 v[132:135], v[92:95], a[172:175], v[132:135]\n"
"ds_read_b128 a[212:215], v27 offset:20480\n"
"s_add_u32 s74, s80, 24576\n"
"v_mfma_f32_16x16x32_bf16 v[136:139], v[96:99], a[172:175], v[136:139]\n"
"buffer_load_dwordx4 a[100:103], v30, s[40:43], s74 offen offset:1024 sc1\n"
"v_mfma_f32_16x16x32_bf16 v[140:143], v[100:103], a[172:175], v[140:143]\n"
"s_waitcnt lgkmcnt(7)\n"
"v_mfma_f32_16x16x32_bf16 v[104:107], v[64:67], a[176:179], v[104:107]\n"
"ds_read_b128 v[84:87], v29 offset:35840\n"
"s_waitcnt lgkmcnt(7)\n"
"v_mfma_f32_16x16x32_bf16 v[108:111], v[68:71], a[176:179], v[108:111]\n"
"ds_read_b128 v[88:91], v29 offset:36096\n"
"s_add_u32 s74, s80, 24576\n"
"s_waitcnt lgkmcnt(7)\n"
"v_mfma_f32_16x16x32_bf16 v[112:115], v[72:75], a[176:179], v[112:115]\n"
"ds_read_b128 v[92:95], v29 offset:36352\n"
"buffer_load_dwordx4 a[104:107], v30, s[40:43], s74 offen offset:2048 sc1\n"
"s_waitcnt lgkmcnt(7)\n"
"v_mfma_f32_16x16x32_bf16 v[116:119], v[76:79], a[176:179], v[116:119]\n"
"ds_read_b128 v[96:99], v29 offset:36608\n"
"s_waitcnt lgkmcnt(7)\n"
"v_mfma_f32_16x16x32_bf16 v[120:123], v[80:83], a[176:179], v[120:123]\n"
"ds_read_b128 v[100:103], v29 offset:36864\n"
"v_mfma_f32_16x16x32_bf16 v[124:127], v[64:67], a[180:183], v[124:127]\n"
"ds_read_b128 a[216:219], v28 offset:16384\n"
"s_add_u32 s74, s80, 24576\n"
"v_mfma_f32_16x16x32_bf16 v[128:131], v[68:71], a[180:183], v[128:131]\n"
"ds_read_b128 a[220:223], v28 offset:20480\n"
"buffer_load_dwordx4 a[108:111], v30, s[40:43], s74 offen offset:3072 sc1\n"
"v_mfma_f32_16x16x32_bf16 v[132:135], v[72:75], a[180:183], v[132:135]\n"
"v_mfma_f32_16x16x32_bf16 v[136:139], v[76:79], a[180:183], v[136:139]\n"
"v_mfma_f32_16x16x32_bf16 v[140:143], v[80:83], a[180:183], v[140:143]\n"
"s_waitcnt lgkmcnt(6)\n"
"v_mfma_f32_16x16x32_bf16 v[104:107], v[84:87], a[184:187], v[104:107]\n"
"s_waitcnt lgkmcnt(5)\n"
"v_mfma_f32_16x16x32_bf16 v[108:111], v[88:91], a[184:187], v[108:111]\n"
"s_waitcnt lgkmcnt(4)\n"
"v_mfma_f32_16x16x32_bf16 v[112:115], v[92:95], a[184:187], v[112:115]\n"
"s_waitcnt lgkmcnt(0)\n"
"s_waitcnt vmcnt(19)\n"
"s_barrier\n"
"v_mfma_f32_16x16x32_bf16 v[116:119], v[96:99], a[184:187], v[116:119]\n"
"ds_read_b128 v[64:67], v51\n"
"v_mfma_f32_16x16x32_bf16 v[120:123], v[100:103], a[184:187], v[120:123]\n"
"ds_read_b128 v[68:71], v51 offset:256\n"
"v_mfma_f32_16x16x32_bf16 v[124:127], v[84:87], a[188:191], v[124:127]\n"
"ds_read_b128 v[72:75], v51 offset:512\n"
"v_mfma_f32_16x16x32_bf16 v[128:131], v[88:91], a[188:191], v[128:131]\n"
"ds_read_b128 v[76:79], v51 offset:768\n"
"v_mfma_f32_16x16x32_bf16 v[132:135], v[92:95], a[188:191], v[132:135]\n"
"ds_read_b128 v[80:83], v51 offset:1024\n"
"s_add_u32 s74, s80, 28672\n"
"v_mfma_f32_16x16x32_bf16 v[136:139], v[96:99], a[188:191], v[136:139]\n"
"buffer_load_dwordx4 a[112:115], v30, s[40:43], s74 offen sc1\n"
"s_add_u32 s74, s80, 28672\n"
"v_mfma_f32_16x16x32_bf16 v[140:143], v[100:103], a[188:191], v[140:143]\n"
"buffer_load_dwordx4 a[116:119], v30, s[40:43], s74 offen offset:1024 sc1\n"
"s_waitcnt vmcnt(17)\n"
"s_waitcnt lgkmcnt(4)\n"
"v_mfma_f32_16x16x32_bf16 v[104:107], v[64:67], a[192:195], v[104:107]\n"
"ds_read_b128 v[84:87], v51 offset:5120\n"
"s_waitcnt lgkmcnt(4)\n"
"v_mfma_f32_16x16x32_bf16 v[108:111], v[68:71], a[192:195], v[108:111]\n"
"ds_read_b128 v[88:91], v51 offset:5376\n"
"s_waitcnt lgkmcnt(4)\n"
"v_mfma_f32_16x16x32_bf16 v[112:115], v[72:75], a[192:195], v[112:115]\n"
"ds_read_b128 v[92:95], v51 offset:5632\n"
"s_waitcnt lgkmcnt(4)\n"
"v_mfma_f32_16x16x32_bf16 v[116:119], v[76:79], a[192:195], v[116:119]\n"
"ds_read_b128 v[96:99], v51 offset:5888\n"
"s_waitcnt lgkmcnt(4)\n"
"v_mfma_f32_16x16x32_bf16 v[120:123], v[80:83], a[192:195], v[120:123]\n"
"ds_read_b128 v[100:103], v51 offset:6144\n"
"v_mfma_f32_16x16x32_bf16 v[124:127], v[64:67], a[196:199], v[124:127]\n"
"ds_read_b128 a[224:227], v25\n"
"v_mfma_f32_16x16x32_bf16 v[128:131], v[68:71], a[196:199], v[128:131]\n"
"ds_read_b128 a[228:231], v25 offset:4096\n"
"v_mfma_f32_16x16x32_bf16 v[132:135], v[72:75], a[196:199], v[132:135]\n"
"ds_read_b128 a[232:235], v26\n"
"v_mfma_f32_16x16x32_bf16 v[136:139], v[76:79], a[196:199], v[136:139]\n"
"s_add_u32 s74, s80, 28672\n"
"v_mfma_f32_16x16x32_bf16 v[140:143], v[80:83], a[196:199], v[140:143]\n"
"buffer_load_dwordx4 a[120:123], v30, s[40:43], s74 offen offset:2048 sc1\n"
"s_waitcnt lgkmcnt(7)\n"
"v_mfma_f32_16x16x32_bf16 v[104:107], v[84:87], a[200:203], v[104:107]\n"
"ds_read_b128 v[64:67], v51 offset:10240\n"
"s_waitcnt lgkmcnt(7)\n"
"v_mfma_f32_16x16x32_bf16 v[108:111], v[88:91], a[200:203], v[108:111]\n"
"ds_read_b128 v[68:71], v51 offset:10496\n"
"s_waitcnt lgkmcnt(7)\n"
"v_mfma_f32_16x16x32_bf16 v[112:115], v[92:95], a[200:203], v[112:115]\n"
"ds_read_b128 v[72:75], v51 offset:10752\n"
"s_waitcnt lgkmcnt(7)\n"
"v_mfma_f32_16x16x32_bf16 v[116:119], v[96:99], a[200:203], v[116:119]\n"
"ds_read_b128 v[76:79], v51 offset:11008\n"
"s_waitcnt lgkmcnt(7)\n"
"v_mfma_f32_16x16x32_bf16 v[120:123], v[100:103], a[200:203], v[120:123]\n"
"ds_read_b128 v[80:83], v51 offset:11264\n"
"v_mfma_f32_16x16x32_bf16 v[124:127], v[84:87], a[204:207], v[124:127]\n"
"ds_read_b128 a[236:239], v26 offset:4096\n"
"v_mfma_f32_16x16x32_bf16 v[128:131], v[88:91], a[204:207], v[128:131]\n"
"ds_read_b128 a[240:243], v27\n"
"v_mfma_f32_16x16x32_bf16 v[132:135], v[92:95], a[204:207], v[132:135]\n"
"ds_read_b128 a[244:247], v27 offset:4096\n"
"v_mfma_f32_16x16x32_bf16 v[136:139], v[96:99], a[204:207], v[136:139]\n"
"s_add_u32 s74, s80, 28672\n"
"v_mfma_f32_16x16x32_bf16 v[140:143], v[100:103], a[204:207], v[140:143]\n"
"buffer_load_dwordx4 a[124:127], v30, s[40:43], s74 offen offset:3072 sc1\n"
"s_waitcnt lgkmcnt(7)\n"
"v_mfma_f32_16x16x32_bf16 v[104:107], v[64:67], a[208:211], v[104:107]\n"
"ds_read_b128 v[84:87], v51 offset:15360\n"
"s_waitcnt lgkmcnt(7)\n"
"v_mfma_f32_16x16x32_bf16 v[108:111], v[68:71], a[208:211], v[108:111]\n"
"ds_read_b128 v[88:91], v51 offset:15616\n"
"s_waitcnt lgkmcnt(7)\n"
"v_mfma_f32_16x16x32_bf16 v[112:115], v[72:75], a[208:211], v[112:115]\n"
"ds_read_b128 v[92:95], v51 offset:15872\n"
"s_waitcnt lgkmcnt(7)\n"
"v_mfma_f32_16x16x32_bf16 v[116:119], v[76:79], a[208:211], v[116:119]\n"
"ds_read_b128 v[96:99], v51 offset:16128\n"
"s_waitcnt lgkmcnt(7)\n"
"v_mfma_f32_16x16x32_bf16 v[120:123], v[80:83], a[208:211], v[120:123]\n"
"ds_read_b128 v[100:103], v51 offset:16384\n"
"v_mfma_f32_16x16x32_bf16 v[124:127], v[64:67], a[212:215], v[124:127]\n"
"ds_read_b128 a[248:251], v28\n"
"v_mfma_f32_16x16x32_bf16 v[128:131], v[68:71], a[212:215], v[128:131]\n"
"ds_read_b128 a[252:255], v28 offset:4096\n"
"v_mfma_f32_16x16x32_bf16 v[132:135], v[72:75], a[212:215], v[132:135]\n"
"s_add_u32 s74, s80, 32768\n"
"v_mfma_f32_16x16x32_bf16 v[136:139], v[76:79], a[212:215], v[136:139]\n"
"buffer_load_dwordx4 a[128:131], v30, s[40:43], s74 offen sc1\n"
"v_mfma_f32_16x16x32_bf16 v[140:143], v[80:83], a[212:215], v[140:143]\n"
"s_waitcnt lgkmcnt(6)\n"
"v_mfma_f32_16x16x32_bf16 v[104:107], v[84:87], a[216:219], v[104:107]\n"
"s_waitcnt lgkmcnt(5)\n"
"v_mfma_f32_16x16x32_bf16 v[108:111], v[88:91], a[216:219], v[108:111]\n"
"s_waitcnt lgkmcnt(4)\n"
"v_mfma_f32_16x16x32_bf16 v[112:115], v[92:95], a[216:219], v[112:115]\n"
"s_waitcnt lgkmcnt(0)\n"
"s_waitcnt vmcnt(10)\n"
"s_barrier\n"
"v_mfma_f32_16x16x32_bf16 v[116:119], v[96:99], a[216:219], v[116:119]\n"
"ds_read_b128 v[64:67], v29\n"
"v_mfma_f32_16x16x32_bf16 v[120:123], v[100:103], a[216:219], v[120:123]\n"
"ds_read_b128 v[68:71], v29 offset:256\n"
"v_mfma_f32_16x16x32_bf16 v[124:127], v[84:87], a[220:223], v[124:127]\n"
"ds_read_b128 v[72:75], v29 offset:512\n"
"v_mfma_f32_16x16x32_bf16 v[128:131], v[88:91], a[220:223], v[128:131]\n"
"ds_read_b128 v[76:79], v29 offset:768\n"
"v_mfma_f32_16x16x32_bf16 v[132:135], v[92:95], a[220:223], v[132:135]\n"
"ds_read_b128 v[80:83], v29 offset:1024\n"
"s_add_u32 s74, s80, 32768\n"
"v_mfma_f32_16x16x32_bf16 v[136:139], v[96:99], a[220:223], v[136:139]\n"
"buffer_load_dwordx4 a[132:135], v30, s[40:43], s74 offen offset:1024 sc1\n"
"v_mfma_f32_16x16x32_bf16 v[140:143], v[100:103], a[220:223], v[140:143]\n"
"s_waitcnt lgkmcnt(4)\n"
"v_mfma_f32_16x16x32_bf16 v[104:107], v[64:67], a[224:227], v[104:107]\n"
"ds_read_b128 v[84:87], v29 offset:5120\n"
"s_waitcnt lgkmcnt(4)\n"
"v_mfma_f32_16x16x32_bf16 v[108:111], v[68:71], a[224:227], v[108:111]\n"
"ds_read_b128 v[88:91], v29 offset:5376\n"
"s_waitcnt lgkmcnt(4)\n"
"v_mfma_f32_16x16x32_bf16 v[112:115], v[72:75], a[224:227], v[112:115]\n"
"ds_read_b128 v[92:95], v29 offset:5632\n"
"s_waitcnt lgkmcnt(4)\n"
"v_mfma_f32_16x16x32_bf16 v[116:119], v[76:79], a[224:227], v[116:119]\n"
"ds_read_b128 v[96:99], v29 offset:5888\n"
"s_waitcnt lgkmcnt(4)\n"
"v_mfma_f32_16x16x32_bf16 v[120:123], v[80:83], a[224:227], v[120:123]\n"
"ds_read_b128 v[100:103], v29 offset:6144\n"
"v_mfma_f32_16x16x32_bf16 v[124:127], v[64:67], a[228:231], v[124:127]\n"
"s_add_u32 s74, s80, 32768\n"
"v_mfma_f32_16x16x32_bf16 v[128:131], v[68:71], a[228:231], v[128:131]\n"
"buffer_load_dwordx4 a[136:139], v30, s[40:43], s74 offen offset:2048 sc1\n"
"v_mfma_f32_16x16x32_bf16 v[132:135], v[72:75], a[228:231], v[132:135]\n"
"v_mfma_f32_16x16x32_bf16 v[136:139], v[76:79], a[228:231], v[136:139]\n"
"s_add_u32 s74, s80, 32768\n"
"v_mfma_f32_16x16x32_bf16 v[140:143], v[80:83], a[228:231], v[140:143]\n"
"buffer_load_dwordx4 a[140:143], v30, s[40:43], s74 offen offset:3072 sc1\n"
"s_waitcnt lgkmcnt(4)\n"
"v_mfma_f32_16x16x32_bf16 v[104:107], v[84:87], a[232:235], v[104:107]\n"
"ds_read_b128 v[64:67], v29 offset:10240\n"
"s_waitcnt lgkmcnt(4)\n"
"v_mfma_f32_16x16x32_bf16 v[108:111], v[88:91], a[232:235], v[108:111]\n"
"ds_read_b128 v[68:71], v29 offset:10496\n"
"s_waitcnt lgkmcnt(4)\n"
"v_mfma_f32_16x16x32_bf16 v[112:115], v[92:95], a[232:235], v[112:115]\n"
"ds_read_b128 v[72:75], v29 offset:10752\n"
"s_waitcnt lgkmcnt(4)\n"
"v_mfma_f32_16x16x32_bf16 v[116:119], v[96:99], a[232:235], v[116:119]\n"
"ds_read_b128 v[76:79], v29 offset:11008\n"
"s_waitcnt lgkmcnt(4)\n"
"v_mfma_f32_16x16x32_bf16 v[120:123], v[100:103], a[232:235], v[120:123]\n"
"ds_read_b128 v[80:83], v29 offset:11264\n"
"v_mfma_f32_16x16x32_bf16 v[124:127], v[84:87], a[236:239], v[124:127]\n"
"v_mfma_f32_16x16x32_bf16 v[128:131], v[88:91], a[236:239], v[128:131]\n"
"v_mfma_f32_16x16x32_bf16 v[132:135], v[92:95], a[236:239], v[132:135]\n"
"s_add_u32 s74, s80, 36864\n"
"v_mfma_f32_16x16x32_bf16 v[136:139], v[96:99], a[236:239], v[136:139]\n"
"buffer_load_dwordx4 a[144:147], v30, s[40:43], s74 offen sc1\n"
"v_mfma_f32_16x16x32_bf16 v[140:143], v[100:103], a[236:239], v[140:143]\n"
"s_waitcnt lgkmcnt(4)\n"
"v_mfma_f32_16x16x32_bf16 v[104:107], v[64:67], a[240:243], v[104:107]\n"
"ds_read_b128 v[84:87], v29 offset:15360\n"
"s_waitcnt lgkmcnt(4)\n"
"v_mfma_f32_16x16x32_bf16 v[108:111], v[68:71], a[240:243], v[108:111]\n"
"ds_read_b128 v[88:91], v29 offset:15616\n"
"s_waitcnt lgkmcnt(4)\n"
"v_mfma_f32_16x16x32_bf16 v[112:115], v[72:75], a[240:243], v[112:115]\n"
"ds_read_b128 v[92:95], v29 offset:15872\n"
"s_waitcnt lgkmcnt(4)\n"
"v_mfma_f32_16x16x32_bf16 v[116:119], v[76:79], a[240:243], v[116:119]\n"
"ds_read_b128 v[96:99], v29 offset:16128\n"
"s_waitcnt lgkmcnt(4)\n"
"v_mfma_f32_16x16x32_bf16 v[120:123], v[80:83], a[240:243], v[120:123]\n"
"ds_read_b128 v[100:103], v29 offset:16384\n"
"v_mfma_f32_16x16x32_bf16 v[124:127], v[64:67], a[244:247], v[124:127]\n"
"s_add_u32 s74, s80, 36864\n"
"v_mfma_f32_16x16x32_bf16 v[128:131], v[68:71], a[244:247], v[128:131]\n"
"buffer_load_dwordx4 a[148:151], v30, s[40:43], s74 offen offset:1024 sc1\n"
"v_mfma_f32_16x16x32_bf16 v[132:135], v[72:75], a[244:247], v[132:135]\n"
"v_mfma_f32_16x16x32_bf16 v[136:139], v[76:79], a[244:247], v[136:139]\n"
"v_mfma_f32_16x16x32_bf16 v[140:143], v[80:83], a[244:247], v[140:143]\n"
"s_waitcnt lgkmcnt(4)\n"
"v_mfma_f32_16x16x32_bf16 v[104:107], v[84:87], a[248:251], v[104:107]\n"
"s_waitcnt lgkmcnt(3)\n"
"v_mfma_f32_16x16x32_bf16 v[108:111], v[88:91], a[248:251], v[108:111]\n"
"s_waitcnt lgkmcnt(2)\n"
"v_mfma_f32_16x16x32_bf16 v[112:115], v[92:95], a[248:251], v[112:115]\n"
"s_waitcnt lgkmcnt(1)\n"
"v_mfma_f32_16x16x32_bf16 v[116:119], v[96:99], a[248:251], v[116:119]\n"
"s_waitcnt lgkmcnt(0)\n"
"v_mfma_f32_16x16x32_bf16 v[120:123], v[100:103], a[248:251], v[120:123]\n"
"v_mfma_f32_16x16x32_bf16 v[124:127], v[84:87], a[252:255], v[124:127]\n"
"v_mfma_f32_16x16x32_bf16 v[128:131], v[88:91], a[252:255], v[128:131]\n"
"v_mfma_f32_16x16x32_bf16 v[132:135], v[92:95], a[252:255], v[132:135]\n"
"v_mfma_f32_16x16x32_bf16 v[136:139], v[96:99], a[252:255], v[136:139]\n"
"v_mfma_f32_16x16x32_bf16 v[140:143], v[100:103], a[252:255], v[140:143]\n"
"s_nop 7\n"
"v_cvt_pk_bf16_f32 v64, v104, v105\n"
"v_cvt_pk_bf16_f32 v65, v106, v107\n"
"v_cvt_pk_bf16_f32 v66, v108, v109\n"
"v_cvt_pk_bf16_f32 v67, v110, v111\n"
"v_cvt_pk_bf16_f32 v68, v112, v113\n"
"v_cvt_pk_bf16_f32 v69, v114, v115\n"
"v_cvt_pk_bf16_f32 v70, v116, v117\n"
"v_cvt_pk_bf16_f32 v71, v118, v119\n"
"v_cvt_pk_bf16_f32 v72, v120, v121\n"
"v_cvt_pk_bf16_f32 v73, v122, v123\n"
"v_cvt_pk_bf16_f32 v74, v124, v125\n"
"v_cvt_pk_bf16_f32 v75, v126, v127\n"
"v_cvt_pk_bf16_f32 v76, v128, v129\n"
"v_cvt_pk_bf16_f32 v77, v130, v131\n"
"v_cvt_pk_bf16_f32 v78, v132, v133\n"
"v_cvt_pk_bf16_f32 v79, v134, v135\n"
"v_cvt_pk_bf16_f32 v80, v136, v137\n"
"v_cvt_pk_bf16_f32 v81, v138, v139\n"
"v_cvt_pk_bf16_f32 v82, v140, v141\n"
"v_cvt_pk_bf16_f32 v83, v142, v143\n"
"s_lshl_b32 s72, s76, 3\n"
"s_add_u32 s72, s72, s77\n"
"s_mul_i32 s72, s72, 20480\n"
"s_mul_i32 s73, s78, 5120\n"
"s_add_u32 s72, s72, s73\n"
"s_add_u32 s72, s72, s79\n"
"buffer_store_dwordx4 v[64:67], v30, s[40:43], s72 offen sc1\n"
"buffer_store_dwordx4 v[68:71], v30, s[40:43], s72 offen offset:1024 sc1\n"
"buffer_store_dwordx4 v[72:75], v30, s[40:43], s72 offen offset:2048 sc1\n"
"buffer_store_dwordx4 v[76:79], v30, s[40:43], s72 offen offset:3072 sc1\n"
"buffer_store_dwordx4 v[80:83], v31, s[40:43], s72 offen sc1\n"
"s_mul_i32 s72, s77, 4\n"
"s_add_u32 s72, s72, s82\n"
"s_waitcnt vmcnt(0)\n"
"s_barrier\n"
"s_cmp_lg_u32 s78, 0\n"
"s_cbranch_scc1 8f\n"
"s_mov_b64 exec, 1\n"
"v_mov_b32 v52, 1\n"
"v_mov_b32 v53, 0\n"
"buffer_store_dword v52, v53, s[40:43], s72 offen sc1\n"
"s_mov_b64 exec, -1\n"
"8:\n"
"s_add_u32 s74, s80, 36864\n"
"buffer_load_dwordx4 a[152:155], v30, s[40:43], s74 offen offset:2048 sc1\n"
"s_add_u32 s74, s80, 36864\n"
"buffer_load_dwordx4 a[156:159], v30, s[40:43], s74 offen offset:3072 sc1\n"
"s_add_u32 s74, s80, 40960\n"
"buffer_load_dwordx4 a[160:163], v30, s[40:43], s74 offen sc1\n"
"s_add_u32 s74, s80, 40960\n"
"buffer_load_dwordx4 a[164:167], v30, s[40:43], s74 offen offset:1024 sc1\n"
"s_add_u32 s74, s80, 40960\n"
"buffer_load_dwordx4 a[168:171], v30, s[40:43], s74 offen offset:2048 sc1\n"
"s_add_u32 s74, s80, 40960\n"
"buffer_load_dwordx4 a[172:175], v30, s[40:43], s74 offen offset:3072 sc1\n"
"s_add_u32 s74, s80, 45056\n"
"buffer_load_dwordx4 a[176:179], v30, s[40:43], s74 offen sc1\n"
"s_add_u32 s74, s80, 45056\n"
"buffer_load_dwordx4 a[180:183], v30, s[40:43], s74 offen offset:1024 sc1\n"
"s_add_u32 s74, s80, 45056\n"
"buffer_load_dwordx4 a[184:187], v30, s[40:43], s74 offen offset:2048 sc1\n"
"s_add_u32 s74, s80, 45056\n"
"buffer_load_dwordx4 a[188:191], v30, s[40:43], s74 offen offset:3072 sc1\n"
"s_add_u32 s74, s80, 49152\n"
"buffer_load_dwordx4 a[192:195], v30, s[40:43], s74 offen sc1\n"
"s_add_u32 s74, s80, 49152\n"
"buffer_load_dwordx4 a[196:199], v30, s[40:43], s74 offen offset:1024 sc1\n"
"s_cmp_lg_u32 s78, 0\n"
"s_cbranch_scc1 38f\n"
"s_cmp_lg_u32 s84, 0\n"
"s_cbranch_scc1 39f\n"
"s_mov_b32 s70, 65536\n"
"buffer_load_dword v56, v48, s[40:43], s82 offen sc1\n"
"31:\n"
"s_waitcnt vmcnt(0)\n"
"v_cmp_ne_u32 vcc, s85, v56\n"
"s_nop 4\n"
"s_cbranch_vccz 39f\n"
"buffer_load_dword v56, v48, s[40:43], s82 offen sc1\n"
"s_sub_u32 s70, s70, 1\n"
"s_cmp_lg_u32 s70, 0\n"
"s_cbranch_scc1 31b\n"
"s_mov_b32 s84, 1\n"
"39:\n"
"38:\n"
"s_barrier\n"
"buffer_load_dwordx4 v[64:67], v30, s[40:43], s56 offen sc1\n"
"buffer_load_dwordx4 v[68:71], v30, s[40:43], s56 offen offset:1024 sc1\n"
"buffer_load_dwordx4 v[72:75], v30, s[40:43], s56 offen offset:2048 sc1\n"
"buffer_load_dwordx4 v[76:79], v30, s[40:43], s56 offen offset:3072 sc1\n"
"buffer_load_dwordx4 v[80:83], v31, s[40:43], s56 offen sc1\n"
"buffer_load_dwordx4 v[84:87], v30, s[40:43], s57 offen sc1\n"
"buffer_load_dwordx4 v[88:91], v30, s[40:43], s57 offen offset:1024 sc1\n"
"buffer_load_dwordx4 v[92:95], v30, s[40:43], s57 offen offset:2048 sc1\n"
"buffer_load_dwordx4 v[96:99], v30, s[40:43], s57 offen offset:3072 sc1\n"
"buffer_load_dwordx4 v[100:103], v31, s[40:43], s57 offen sc1\n"
"s_add_u32 s73, s71, 1\n"
"s_and_b32 s72, s73, 1\n"
"s_cmp_eq_u32 s72, 0\n"
"s_cselect_b64 s[44:45], s[20:21], s[24:25]\n"
"s_cselect_b64 s[48:49], s[22:23], s[26:27]\n"
"s_cmp_lt_u32 s73, 2\n"
"s_cselect_b32 s75, 2, 0\n"
"s_mul_i32 s75, s75, s77\n"
"s_lshl_b32 s75, s75, 8\n"
"s_and_b32 s75, s75, 0xfff\n"
"s_mov_b32 s65, s75\n"
"s_waitcnt vmcnt(9)\n"
"v_mfma_f32_16x16x32_bf16 a[200:203], v[64:67], a[0:3], 0\n"
"v_mfma_f32_16x16x32_bf16 a[204:207], v[64:67], a[4:7], 0\n"
"v_mfma_f32_16x16x32_bf16 a[208:211], v[64:67], a[8:11], 0\n"
"v_mfma_f32_16x16x32_bf16 a[212:215], v[64:67], a[12:15], 0\n"
"v_mfma_f32_16x16x32_bf16 a[216:219], v[64:67], a[16:19], 0\n"
"buffer_load_dwordx4 v[104:107], v30, s[40:43], s58 offen sc1\n"
"s_waitcnt vmcnt(9)\n"
"v_mfma_f32_16x16x32_bf16 a[200:203], v[68:71], a[20:23], a[200:203]\n"
"v_mfma_f32_16x16x32_bf16 a[204:207], v[68:71], a[24:27], a[204:207]\n"
"v_mfma_f32_16x16x32_bf16 a[208:211], v[68:71], a[28:31], a[208:211]\n"
"v_mfma_f32_16x16x32_bf16 a[212:215], v[68:71], a[32:35], a[212:215]\n"
"v_mfma_f32_16x16x32_bf16 a[216:219], v[68:71], a[36:39], a[216:219]\n"
"buffer_load_dwordx4 v[108:111], v30, s[40:43], s58 offen offset:1024 sc1\n"
"s_waitcnt vmcnt(9)\n"
"v_mfma_f32_16x16x32_bf16 a[200:203], v[72:75], a[40:43], a[200:203]\n"
"v_mfma_f32_16x16x32_bf16 a[204:207], v[72:75], a[44:47], a[204:207]\n"
"v_mfma_f32_16x16x32_bf16 a[208:211], v[72:75], a[48:51], a[208:211]\n"
"v_mfma_f32_16x16x32_bf16 a[212:215], v[72:75], a[52:55], a[212:215]\n"
"v_mfma_f32_16x16x32_bf16 a[216:219], v[72:75], a[56:59], a[216:219]\n"
"buffer_load_dwordx4 v[112:115], v30, s[40:43], s58 offen offset:2048 sc1\n"
"s_waitcnt vmcnt(9)\n"
"v_mfma_f32_16x16x32_bf16 a[200:203], v[76:79], a[60:63], a[200:203]\n"
"v_mfma_f32_16x16x32_bf16 a[204:207], v[76:79], a[64:67], a[204:207]\n"
"v_mfma_f32_16x16x32_bf16 a[208:211], v[76:79], a[68:71], a[208:211]\n"
"v_mfma_f32_16x16x32_bf16 a[212:215], v[76:79], a[72:75], a[212:215]\n"
"v_mfma_f32_16x16x32_bf16 a[216:219], v[76:79], a[76:79], a[216:219]\n"
"buffer_load_dwordx4 v[116:119], v30, s[40:43], s58 offen offset:3072 sc1\n"
"s_waitcnt vmcnt(9)\n"
"v_mfma_f32_16x16x32_bf16 a[200:203], v[80:83], a[80:83], a[200:203]\n"
"v_mfma_f32_16x16x32_bf16 a[204:207], v[80:83], a[84:87], a[204:207]\n"
"v_mfma_f32_16x16x32_bf16 a[208:211], v[80:83], a[88:91], a[208:211]\n"
"v_mfma_f32_16x16x32_bf16 a[212:215], v[80:83], a[92:95], a[212:215]\n"
"v_mfma_f32_16x16x32_bf16 a[216:219], v[80:83], a[96:99], a[216:219]\n"
"buffer_load_dwordx4 v[120:123], v31, s[40:43], s58 offen sc1\n"
"s_waitcnt vmcnt(9)\n"
"v_mfma_f32_16x16x32_bf16 a[200:203], v[84:87], a[100:103], a[200:203]\n"
"v_mfma_f32_16x16x32_bf16 a[204:207], v[84:87], a[104:107], a[204:207]\n"
"v_mfma_f32_16x16x32_bf16 a[208:211], v[84:87], a[108:111], a[208:211]\n"
"v_mfma_f32_16x16x32_bf16 a[212:215], v[84:87], a[112:115], a[212:215]\n"
"v_mfma_f32_16x16x32_bf16 a[216:219], v[84:87], a[116:119], a[216:219]\n"
"buffer_load_dwordx4 v[124:127], v30, s[40:43], s59 offen sc1\n"
"s_waitcnt vmcnt(9)\n"
"v_mfma_f32_16x16x32_bf16 a[200:203], v[88:91], a[120:123], a[200:203]\n"
"v_mfma_f32_16x16x32_bf16 a[204:207], v[88:91], a[124:127], a[204:207]\n"
"v_mfma_f32_16x16x32_bf16 a[208:211], v[88:91], a[128:131], a[208:211]\n"
"v_mfma_f32_16x16x32_bf16 a[212:215], v[88:91], a[132:135], a[212:215]\n"
"v_mfma_f32_16x16x32_bf16 a[216:219], v[88:91], a[136:139], a[216:219]\n"
"buffer_load_dwordx4 v[128:131], v30, s[40:43], s59 offen offset:1024 sc1\n"
"s_waitcnt vmcnt(9)\n"
"v_mfma_f32_16x16x32_bf16 a[200:203], v[92:95], a[140:143], a[200:203]\n"
"v_mfma_f32_16x16x32_bf16 a[204:207], v[92:95], a[144:147], a[204:207]\n"
"v_mfma_f32_16x16x32_bf16 a[208:211], v[92:95], a[148:151], a[208:211]\n"
"v_mfma_f32_16x16x32_bf16 a[212:215], v[92:95], a[152:155], a[212:215]\n"
"v_mfma_f32_16x16x32_bf16 a[216:219], v[92:95], a[156:159], a[216:219]\n"
"buffer_load_dwordx4 v[132:135], v30, s[40:43], s59 offen offset:2048 sc1\n"
"s_waitcnt vmcnt(9)\n"
"v_mfma_f32_16x16x32_bf16 a[200:203], v[96:99], a[160:163], a[200:203]\n"
"v_mfma_f32_16x16x32_bf16 a[204:207], v[96:99], a[164:167], a[204:207]\n"
"v_mfma_f32_16x16x32_bf16 a[208:211], v[96:99], a[168:171], a[208:211]\n"
"v_mfma_f32_16x16x32_bf16 a[212:215], v[96:99], a[172:175], a[212:215]\n"
"v_mfma_f32_16x16x32_bf16 a[216:219], v[96:99], a[176:179], a[216:219]\n"
"buffer_load_dwordx4 v[136:139], v30, s[40:43], s59 offen offset:3072 sc1\n"
"s_waitcnt vmcnt(9)\n"
"v_mfma_f32_16x16x32_bf16 a[200:203], v[100:103], a[180:183], a[200:203]\n"
"v_mfma_f32_16x16x32_bf16 a[204:207], v[100:103], a[184:187], a[204:207]\n"
"v_mfma_f32_16x16x32_bf16 a[208:211], v[100:103], a[188:191], a[208:211]\n"
"v_mfma_f32_16x16x32_bf16 a[212:215], v[100:103], a[192:195], a[212:215]\n"
"v_mfma_f32_16x16x32_bf16 a[216:219], v[100:103], a[196:199], a[216:219]\n"
"buffer_load_dwordx4 v[140:143], v31, s[40:43], s59 offen sc1\n"
"s_waitcnt vmcnt(9)\n"
"v_mfma_f32_16x16x32_bf16 a[220:223], v[104:107], a[0:3], 0\n"
"v_mfma_f32_16x16x32_bf16 a[224:227], v[104:107], a[4:7], 0\n"
"v_mfma_f32_16x16x32_bf16 a[228:231], v[104:107], a[8:11], 0\n"
"v_mfma_f32_16x16x32_bf16 a[232:235], v[104:107], a[12:15], 0\n"
"v_mfma_f32_16x16x32_bf16 a[236:239], v[104:107], a[16:19], 0\n"
"buffer_load_dwordx4 v[144:147], v30, s[40:43], s60 offen sc1\n"
"s_waitcnt vmcnt(9)\n"
"v_mfma_f32_16x16x32_bf16 a[220:223], v[108:111], a[20:23], a[220:223]\n"
"v_mfma_f32_16x16x32_bf16 a[224:227], v[108:111], a[24:27], a[224:227]\n"
"v_mfma_f32_16x16x32_bf16 a[228:231], v[108:111], a[28:31], a[228:231]\n"
"v_mfma_f32_16x16x32_bf16 a[232:235], v[108:111], a[32:35], a[232:235]\n"
"v_mfma_f32_16x16x32_bf16 a[236:239], v[108:111], a[36:39], a[236:239]\n"
"buffer_load_dwordx4 v[148:151], v30, s[40:43], s60 offen offset:1024 sc1\n"
"ds_write_b128 v47, a[200:203] offset:0\n"
"s_waitcnt vmcnt(9)\n"
"v_mfma_f32_16x16x32_bf16 a[220:223], v[112:115], a[40:43], a[220:223]\n"
"ds_write_b128 v47, a[204:207] offset:1024\n"
"v_mfma_f32_16x16x32_bf16 a[224:227], v[112:115], a[44:47], a[224:227]\n"
"ds_write_b128 v47, a[208:211] offset:2048\n"
"v_mfma_f32_16x16x32_bf16 a[228:231], v[112:115], a[48:51], a[228:231]\n"
"ds_write_b128 v47, a[212:215] offset:3072\n"
"v_mfma_f32_16x16x32_bf16 a[232:235], v[112:115], a[52:55], a[232:235]\n"
"ds_write_b128 v47, a[216:219] offset:4096\n"
"v_mfma_f32_16x16x32_bf16 a[236:239], v[112:115], a[56:59], a[236:239]\n"
"buffer_load_dwordx4 v[152:155], v30, s[40:43], s60 offen offset:2048 sc1\n"
"s_waitcnt vmcnt(9)\n"
"v_mfma_f32_16x16x32_bf16 a[220:223], v[116:119], a[60:63], a[220:223]\n"
"v_mfma_f32_16x16x32_bf16 a[224:227], v[116:119], a[64:67], a[224:227]\n"
"v_mfma_f32_16x16x32_bf16 a[228:231], v[116:119], a[68:71], a[228:231]\n"
"v_mfma_f32_16x16x32_bf16 a[232:235], v[116:119], a[72:75], a[232:235]\n"
"v_mfma_f32_16x16x32_bf16 a[236:239], v[116:119], a[76:79], a[236:239]\n"
"buffer_load_dwordx4 v[156:159], v30, s[40:43], s60 offen offset:3072 sc1\n"
"s_waitcnt vmcnt(9)\n"
"v_mfma_f32_16x16x32_bf16 a[220:223], v[120:123], a[80:83], a[220:223]\n"
"v_mfma_f32_16x16x32_bf16 a[224:227], v[120:123], a[84:87], a[224:227]\n"
"v_mfma_f32_16x16x32_bf16 a[228:231], v[120:123], a[88:91], a[228:231]\n"
"v_mfma_f32_16x16x32_bf16 a[232:235], v[120:123], a[92:95], a[232:235]\n"
"v_mfma_f32_16x16x32_bf16 a[236:239], v[120:123], a[96:99], a[236:239]\n"
"buffer_load_dwordx4 v[160:163], v31, s[40:43], s60 offen sc1\n"
"s_waitcnt vmcnt(9)\n"
"v_mfma_f32_16x16x32_bf16 a[220:223], v[124:127], a[100:103], a[220:223]\n"
"v_mfma_f32_16x16x32_bf16 a[224:227], v[124:127], a[104:107], a[224:227]\n"
"v_mfma_f32_16x16x32_bf16 a[228:231], v[124:127], a[108:111], a[228:231]\n"
"v_mfma_f32_16x16x32_bf16 a[232:235], v[124:127], a[112:115], a[232:235]\n"
"v_mfma_f32_16x16x32_bf16 a[236:239], v[124:127], a[116:119], a[236:239]\n"
"buffer_load_dwordx4 v[164:167], v30, s[40:43], s61 offen sc1\n"
"s_waitcnt vmcnt(9)\n"
"v_mfma_f32_16x16x32_bf16 a[220:223], v[128:131], a[120:123], a[220:223]\n"
"v_mfma_f32_16x16x32_bf16 a[224:227], v[128:131], a[124:127], a[224:227]\n"
"v_mfma_f32_16x16x32_bf16 a[228:231], v[128:131], a[128:131], a[228:231]\n"
"v_mfma_f32_16x16x32_bf16 a[232:235], v[128:131], a[132:135], a[232:235]\n"
"v_mfma_f32_16x16x32_bf16 a[236:239], v[128:131], a[136:139], a[236:239]\n"
"buffer_load_dwordx4 v[168:171], v30, s[40:43], s61 offen offset:1024 sc1\n"
"s_waitcnt vmcnt(9)\n"
"v_mfma_f32_16x16x32_bf16 a[220:223], v[132:135], a[140:143], a[220:223]\n"
"v_mfma_f32_16x16x32_bf16 a[224:227], v[132:135], a[144:147], a[224:227]\n"
"v_mfma_f32_16x16x32_bf16 a[228:231], v[132:135], a[148:151], a[228:231]\n"
"v_mfma_f32_16x16x32_bf16 a[232:235], v[132:135], a[152:155], a[232:235]\n"
"v_mfma_f32_16x16x32_bf16 a[236:239], v[132:135], a[156:159], a[236:239]\n"
"buffer_load_dwordx4 v[172:175], v30, s[40:43], s61 offen offset:2048 sc1\n"
"s_waitcnt vmcnt(9)\n"
"v_mfma_f32_16x16x32_bf16 a[220:223], v[136:139], a[160:163], a[220:223]\n"
"v_mfma_f32_16x16x32_bf16 a[224:227], v[136:139], a[164:167], a[224:227]\n"
"v_mfma_f32_16x16x32_bf16 a[228:231], v[136:139], a[168:171], a[228:231]\n"
"v_mfma_f32_16x16x32_bf16 a[232:235], v[136:139], a[172:175], a[232:235]\n"
"v_mfma_f32_16x16x32_bf16 a[236:239], v[136:139], a[176:179], a[236:239]\n"
"buffer_load_dwordx4 v[176:179], v30, s[40:43], s61 offen offset:3072 sc1\n"
"s_waitcnt vmcnt(9)\n"
"v_mfma_f32_16x16x32_bf16 a[220:223], v[140:143], a[180:183], a[220:223]\n"
"v_mfma_f32_16x16x32_bf16 a[224:227], v[140:143], a[184:187], a[224:227]\n"
"v_mfma_f32_16x16x32_bf16 a[228:231], v[140:143], a[188:191], a[228:231]\n"
"v_mfma_f32_16x16x32_bf16 a[232:235], v[140:143], a[192:195], a[232:235]\n"
"v_mfma_f32_16x16x32_bf16 a[236:239], v[140:143], a[196:199], a[236:239]\n"
"buffer_load_dwordx4 v[180:183], v31, s[40:43], s61 offen sc1\n"
"s_waitcnt vmcnt(9)\n"
"v_mfma_f32_16x16x32_bf16 a[200:203], v[144:147], a[0:3], 0\n"
"v_mfma_f32_16x16x32_bf16 a[204:207], v[144:147], a[4:7], 0\n"
"v_mfma_f32_16x16x32_bf16 a[208:211], v[144:147], a[8:11], 0\n"
"v_mfma_f32_16x16x32_bf16 a[212:215], v[144:147], a[12:15], 0\n"
"v_mfma_f32_16x16x32_bf16 a[216:219], v[144:147], a[16:19], 0\n"
"buffer_load_dwordx4 v[184:187], v30, s[40:43], s62 offen sc1\n"
"s_waitcnt vmcnt(9)\n"
"v_mfma_f32_16x16x32_bf16 a[200:203], v[148:151], a[20:23], a[200:203]\n"
"v_mfma_f32_16x16x32_bf16 a[204:207], v[148:151], a[24:27], a[204:207]\n"
"v_mfma_f32_16x16x32_bf16 a[208:211], v[148:151], a[28:31], a[208:211]\n"
"v_mfma_f32_16x16x32_bf16 a[212:215], v[148:151], a[32:35], a[212:215]\n"
"v_mfma_f32_16x16x32_bf16 a[216:219], v[148:151], a[36:39], a[216:219]\n"
"buffer_load_dwordx4 v[188:191], v30, s[40:43], s62 offen offset:1024 sc1\n"
"ds_write_b128 v47, a[220:223] offset:5120\n"
"s_waitcnt vmcnt(9)\n"
"v_mfma_f32_16x16x32_bf16 a[200:203], v[152:155], a[40:43], a[200:203]\n"
"ds_write_b128 v47, a[224:227] offset:6144\n"
"v_mfma_f32_16x16x32_bf16 a[204:207], v[152:155], a[44:47], a[204:207]\n"
"ds_write_b128 v47, a[228:231] offset:7168\n"
"v_mfma_f32_16x16x32_bf16 a[208:211], v[152:155], a[48:51], a[208:211]\n"
"ds_write_b128 v47, a[232:235] offset:8192\n"
"v_mfma_f32_16x16x32_bf16 a[212:215], v[152:155], a[52:55], a[212:215]\n"
"ds_write_b128 v47, a[236:239] offset:9216\n"
"v_mfma_f32_16x16x32_bf16 a[216:219], v[152:155], a[56:59], a[216:219]\n"
"buffer_load_dwordx4 v[192:195], v30, s[40:43], s62 offen offset:2048 sc1\n"
"s_waitcnt vmcnt(9)\n"
"v_mfma_f32_16x16x32_bf16 a[200:203], v[156:159], a[60:63], a[200:203]\n"
"v_mfma_f32_16x16x32_bf16 a[204:207], v[156:159], a[64:67], a[204:207]\n"
"v_mfma_f32_16x16x32_bf16 a[208:211], v[156:159], a[68:71], a[208:211]\n"
"v_mfma_f32_16x16x32_bf16 a[212:215], v[156:159], a[72:75], a[212:215]\n"
"v_mfma_f32_16x16x32_bf16 a[216:219], v[156:159], a[76:79], a[216:219]\n"
"buffer_load_dwordx4 v[196:199], v30, s[40:43], s62 offen offset:3072 sc1\n"
"s_waitcnt vmcnt(9)\n"
"v_mfma_f32_16x16x32_bf16 a[200:203], v[160:163], a[80:83], a[200:203]\n"
"v_mfma_f32_16x16x32_bf16 a[204:207], v[160:163], a[84:87], a[204:207]\n"
"v_mfma_f32_16x16x32_bf16 a[208:211], v[160:163], a[88:91], a[208:211]\n"
"v_mfma_f32_16x16x32_bf16 a[212:215], v[160:163], a[92:95], a[212:215]\n"
"v_mfma_f32_16x16x32_bf16 a[216:219], v[160:163], a[96:99], a[216:219]\n"
"buffer_load_dwordx4 v[200:203], v31, s[40:43], s62 offen sc1\n"
"s_waitcnt vmcnt(9)\n"
"v_mfma_f32_16x16x32_bf16 a[200:203], v[164:167], a[100:103], a[200:203]\n"
"v_mfma_f32_16x16x32_bf16 a[204:207], v[164:167], a[104:107], a[204:207]\n"
"v_mfma_f32_16x16x32_bf16 a[208:211], v[164:167], a[108:111], a[208:211]\n"
"v_mfma_f32_16x16x32_bf16 a[212:215], v[164:167], a[112:115], a[212:215]\n"
"v_mfma_f32_16x16x32_bf16 a[216:219], v[164:167], a[116:119], a[216:219]\n"
"buffer_load_dwordx4 v[204:207], v30, s[40:43], s63 offen sc1\n"
"s_waitcnt vmcnt(9)\n"
"v_mfma_f32_16x16x32_bf16 a[200:203], v[168:171], a[120:123], a[200:203]\n"
"v_mfma_f32_16x16x32_bf16 a[204:207], v[168:171], a[124:127], a[204:207]\n"
"v_mfma_f32_16x16x32_bf16 a[208:211], v[168:171], a[128:131], a[208:211]\n"
"v_mfma_f32_16x16x32_bf16 a[212:215], v[168:171], a[132:135], a[212:215]\n"
"v_mfma_f32_16x16x32_bf16 a[216:219], v[168:171], a[136:139], a[216:219]\n"
"buffer_load_dwordx4 v[208:211], v30, s[40:43], s63 offen offset:1024 sc1\n"
"s_waitcnt vmcnt(9)\n"
"v_mfma_f32_16x16x32_bf16 a[200:203], v[172:175], a[140:143], a[200:203]\n"
"v_mfma_f32_16x16x32_bf16 a[204:207], v[172:175], a[144:147], a[204:207]\n"
"v_mfma_f32_16x16x32_bf16 a[208:211], v[172:175], a[148:151], a[208:211]\n"
"v_mfma_f32_16x16x32_bf16 a[212:215], v[172:175], a[152:155], a[212:215]\n"
"v_mfma_f32_16x16x32_bf16 a[216:219], v[172:175], a[156:159], a[216:219]\n"
"buffer_load_dwordx4 v[212:215], v30, s[40:43], s63 offen offset:2048 sc1\n"
"s_waitcnt vmcnt(9)\n"
"v_mfma_f32_16x16x32_bf16 a[200:203], v[176:179], a[160:163], a[200:203]\n"
"v_mfma_f32_16x16x32_bf16 a[204:207], v[176:179], a[164:167], a[204:207]\n"
"v_mfma_f32_16x16x32_bf16 a[208:211], v[176:179], a[168:171], a[208:211]\n"
"v_mfma_f32_16x16x32_bf16 a[212:215], v[176:179], a[172:175], a[212:215]\n"
"v_mfma_f32_16x16x32_bf16 a[216:219], v[176:179], a[176:179], a[216:219]\n"
"buffer_load_dwordx4 v[216:219], v30, s[40:43], s63 offen offset:3072 sc1\n"
"s_waitcnt vmcnt(9)\n"
"v_mfma_f32_16x16x32_bf16 a[200:203], v[180:183], a[180:183], a[200:203]\n"
"v_mfma_f32_16x16x32_bf16 a[204:207], v[180:183], a[184:187], a[204:207]\n"
"v_mfma_f32_16x16x32_bf16 a[208:211], v[180:183], a[188:191], a[208:211]\n"
"v_mfma_f32_16x16x32_bf16 a[212:215], v[180:183], a[192:195], a[212:215]\n"
"v_mfma_f32_16x16x32_bf16 a[216:219], v[180:183], a[196:199], a[216:219]\n"
"buffer_load_dwordx4 v[220:223], v31, s[40:43], s63 offen sc1\n"
"s_waitcnt vmcnt(9)\n"
"v_mfma_f32_16x16x32_bf16 v[224:227], v[184:187], a[0:3], 0\n"
"v_mfma_f32_16x16x32_bf16 v[228:231], v[184:187], a[4:7], 0\n"
"v_mfma_f32_16x16x32_bf16 v[232:235], v[184:187], a[8:11], 0\n"
"v_mfma_f32_16x16x32_bf16 v[236:239], v[184:187], a[12:15], 0\n"
"v_mfma_f32_16x16x32_bf16 v[240:243], v[184:187], a[16:19], 0\n"
"s_waitcnt vmcnt(8)\n"
"v_mfma_f32_16x16x32_bf16 v[224:227], v[188:191], a[20:23], v[224:227]\n"
"v_mfma_f32_16x16x32_bf16 v[228:231], v[188:191], a[24:27], v[228:231]\n"
"v_mfma_f32_16x16x32_bf16 v[232:235], v[188:191], a[28:31], v[232:235]\n"
"v_mfma_f32_16x16x32_bf16 v[236:239], v[188:191], a[32:35], v[236:239]\n"
"v_mfma_f32_16x16x32_bf16 v[240:243], v[188:191], a[36:39], v[240:243]\n"
"ds_write_b128 v47, a[200:203] offset:10240\n"
"s_waitcnt vmcnt(7)\n"
"v_mfma_f32_16x16x32_bf16 v[224:227], v[192:195], a[40:43], v[224:227]\n"
"ds_write_b128 v47, a[204:207] offset:11264\n"
"v_mfma_f32_16x16x32_bf16 v[228:231], v[192:195], a[44:47], v[228:231]\n"
"ds_write_b128 v47, a[208:211] offset:12288\n"
"v_mfma_f32_16x16x32_bf16 v[232:235], v[192:195], a[48:51], v[232:235]\n"
"ds_write_b128 v47, a[212:215] offset:13312\n"
"v_mfma_f32_16x16x32_bf16 v[236:239], v[192:195], a[52:55], v[236:239]\n"
"ds_write_b128 v47, a[216:219] offset:14336\n"
"v_mfma_f32_16x16x32_bf16 v[240:243], v[192:195], a[56:59], v[240:243]\n"
"s_waitcnt vmcnt(6)\n"
"v_mfma_f32_16x16x32_bf16 v[224:227], v[196:199], a[60:63], v[224:227]\n"
"v_mfma_f32_16x16x32_bf16 v[228:231], v[196:199], a[64:67], v[228:231]\n"
"v_mfma_f32_16x16x32_bf16 v[232:235], v[196:199], a[68:71], v[232:235]\n"
"v_mfma_f32_16x16x32_bf16 v[236:239], v[196:199], a[72:75], v[236:239]\n"
"v_mfma_f32_16x16x32_bf16 v[240:243], v[196:199], a[76:79], v[240:243]\n"
"s_waitcnt vmcnt(5)\n"
"v_mfma_f32_16x16x32_bf16 v[224:227], v[200:203], a[80:83], v[224:227]\n"
"s_add_u32 m0, s64, 0\n"
"v_mfma_f32_16x16x32_bf16 v[228:231], v[200:203], a[84:87], v[228:231]\n"
"buffer_load_dwordx4 v16, s[44:47], s65 offen lds\n"
"v_mfma_f32_16x16x32_bf16 v[232:235], v[200:203], a[88:91], v[232:235]\n"
"buffer_load_dwordx4 v17, s[44:47], s65 offen offset:1024 lds\n"
"v_mfma_f32_16x16x32_bf16 v[236:239], v[200:203], a[92:95], v[236:239]\n"
"buffer_load_dwordx4 v18, s[44:47], s65 offen offset:2048 lds\n"
"v_mfma_f32_16x16x32_bf16 v[240:243], v[200:203], a[96:99], v[240:243]\n"
"buffer_load_dwordx4 v19, s[44:47], s65 offen offset:3072 lds\n"
"s_waitcnt vmcnt(8)\n"
"v_mfma_f32_16x16x32_bf16 v[224:227], v[204:207], a[100:103], v[224:227]\n"
"s_add_u32 m0, s64, 4096\n"
"v_mfma_f32_16x16x32_bf16 v[228:231], v[204:207], a[104:107], v[228:231]\n"
"buffer_load_dwordx4 v16, s[48:51], s65 offen lds\n"
"v_mfma_f32_16x16x32_bf16 v[232:235], v[204:207], a[108:111], v[232:235]\n"
"buffer_load_dwordx4 v17, s[48:51], s65 offen offset:1024 lds\n"
"v_mfma_f32_16x16x32_bf16 v[236:239], v[204:207], a[112:115], v[236:239]\n"
"buffer_load_dwordx4 v18, s[48:51], s65 offen offset:2048 lds\n"
"v_mfma_f32_16x16x32_bf16 v[240:243], v[204:207], a[116:119], v[240:243]\n"
"buffer_load_dwordx4 v19, s[48:51], s65 offen offset:3072 lds\n"
"s_waitcnt vmcnt(11)\n"
"v_mfma_f32_16x16x32_bf16 v[224:227], v[208:211], a[120:123], v[224:227]\n"
"s_add_u32 s65, s65, 256\n"
"v_mfma_f32_16x16x32_bf16 v[228:231], v[208:211], a[124:127], v[228:231]\n"
"s_and_b32 s65, s65, 0xfff\n"
"v_mfma_f32_16x16x32_bf16 v[232:235], v[208:211], a[128:131], v[232:235]\n"
"v_mfma_f32_16x16x32_bf16 v[236:239], v[208:211], a[132:135], v[236:239]\n"
"v_mfma_f32_16x16x32_bf16 v[240:243], v[208:211], a[136:139], v[240:243]\n"
"s_waitcnt vmcnt(10)\n"
"v_mfma_f32_16x16x32_bf16 v[224:227], v[212:215], a[140:143], v[224:227]\n"
"v_mfma_f32_16x16x32_bf16 v[228:231], v[212:215], a[144:147], v[228:231]\n"
"v_mfma_f32_16x16x32_bf16 v[232:235], v[212:215], a[148:151], v[232:235]\n"
"v_mfma_f32_16x16x32_bf16 v[236:239], v[212:215], a[152:155], v[236:239]\n"
"v_mfma_f32_16x16x32_bf16 v[240:243], v[212:215], a[156:159], v[240:243]\n"
"s_waitcnt vmcnt(9)\n"
"v_mfma_f32_16x16x32_bf16 v[224:227], v[216:219], a[160:163], v[224:227]\n"
"v_mfma_f32_16x16x32_bf16 v[228:231], v[216:219], a[164:167], v[228:231]\n"
"v_mfma_f32_16x16x32_bf16 v[232:235], v[216:219], a[168:171], v[232:235]\n"
"v_mfma_f32_16x16x32_bf16 v[236:239], v[216:219], a[172:175], v[236:239]\n"
"v_mfma_f32_16x16x32_bf16 v[240:243], v[216:219], a[176:179], v[240:243]\n"
"s_waitcnt vmcnt(8)\n"
"v_mfma_f32_16x16x32_bf16 v[224:227], v[220:223], a[180:183], v[224:227]\n"
"v_mfma_f32_16x16x32_bf16 v[228:231], v[220:223], a[184:187], v[228:231]\n"
"v_mfma_f32_16x16x32_bf16 v[232:235], v[220:223], a[188:191], v[232:235]\n"
"v_mfma_f32_16x16x32_bf16 v[236:239], v[220:223], a[192:195], v[236:239]\n"
"v_mfma_f32_16x16x32_bf16 v[240:243], v[220:223], a[196:199], v[240:243]\n"
"s_cmp_eq_u32 s71, 3\n"
"s_cbranch_scc1 5f\n"
"s_waitcnt lgkmcnt(0)\n"
"s_barrier\n"
"s_add_u32 s72, s78, 1\n"
"s_and_b32 s72, s72, 3\n"
"s_mul_i32 s72, s72, 15360\n"
"s_add_u32 s72, s72, 108544\n"
"s_add_u32 s72, s72, s96\n"
"s_add_u32 m0, s64, 8192\n"
"v_add_u32 v53, s72, v30\n"
"s_add_u32 s72, s78, 2\n"
"s_and_b32 s72, s72, 3\n"
"s_mul_i32 s72, s72, 15360\n"
"buffer_load_dwordx4 v16, s[44:47], s65 offen lds\n"
"s_add_u32 s72, s72, 103424\n"
"s_add_u32 s72, s72, s96\n"
"v_add_u32 v54, s72, v30\n"
"s_add_u32 s72, s78, 3\n"
"buffer_load_dwordx4 v17, s[44:47], s65 offen offset:1024 lds\n"
"s_and_b32 s72, s72, 3\n"
"s_mul_i32 s72, s72, 15360\n"
"s_add_u32 s72, s72, 98304\n"
"s_add_u32 s72, s72, s96\n"
"buffer_load_dwordx4 v18, s[44:47], s65 offen offset:2048 lds\n"
"v_add_u32 v55, s72, v30\n"
"ds_read_b128 v[64:67], v53\n"
"ds_read_b128 v[68:71], v53 offset:1024\n"
"ds_read_b128 v[72:75], v53 offset:2048\n"
"buffer_load_dwordx4 v19, s[44:47], s65 offen offset:3072 lds\n"
"ds_read_b128 v[76:79], v53 offset:3072\n"
"ds_read_b128 v[80:83], v53 offset:4096\n"
"ds_read_b128 v[84:87], v54\n"
"ds_read_b128 v[88:91], v54 offset:1024\n"
"s_add_u32 m0, s64, 12288\n"
"ds_read_b128 v[92:95], v54 offset:2048\n"
"ds_read_b128 v[96:99], v54 offset:3072\n"
"ds_read_b128 v[100:103], v54 offset:4096\n"
"ds_read_b128 v[104:107], v55\n"
"buffer_load_dwordx4 v16, s[48:51], s65 offen lds\n"
"ds_read_b128 v[108:111], v55 offset:1024\n"
"ds_read_b128 v[112:115], v55 offset:2048\n"
"ds_read_b128 v[116:119], v55 offset:3072\n"
"ds_read_b128 v[120:123], v55 offset:4096\n"
"buffer_load_dwordx4 v17, s[48:51], s65 offen offset:1024 lds\n"
"v_lshlrev_b32 v124, 16, v32\n"
"v_and_b32 v125, 0xffff0000, v32\n"
"v_lshlrev_b32 v126, 16, v33\n"
"v_and_b32 v127, 0xffff0000, v33\n"
"buffer_load_dwordx4 v18, s[48:51], s65 offen offset:2048 lds\n"
"v_lshlrev_b32 v128, 16, v34\n"
"v_and_b32 v129, 0xffff0000, v34\n"
"v_lshlrev_b32 v130, 16, v35\n"
"v_and_b32 v131, 0xffff0000, v35\n"
"buffer_load_dwordx4 v19, s[48:51], s65 offen offset:3072 lds\n"
"v_lshlrev_b32 v132, 16, v36\n"
"v_and_b32 v133, 0xffff0000, v36\n"
"v_lshlrev_b32 v134, 16, v37\n"
"v_and_b32 v135, 0xffff0000, v37\n"
"s_add_u32 s65, s65, 256\n"
"v_lshlrev_b32 v136, 16, v38\n"
"v_and_b32 v137, 0xffff0000, v38\n"
"v_lshlrev_b32 v138, 16, v39\n"
"v_and_b32 v139, 0xffff0000, v39\n"
"s_and_b32 s65, s65, 0xfff\n"
"v_lshlrev_b32 v140, 16, v40\n"
"v_and_b32 v141, 0xffff0000, v40\n"
"v_lshlrev_b32 v142, 16, v41\n"
"v_and_b32 v143, 0xffff0000, v41\n"
"s_nop 7\n"
"s_waitcnt lgkmcnt(14)\n"
"v_pk_add_f32 v[224:225], v[224:225], v[64:65]\n"
"v_pk_add_f32 v[226:227], v[226:227], v[66:67]\n"
"s_waitcnt lgkmcnt(13)\n"
"v_pk_add_f32 v[228:229], v[228:229], v[68:69]\n"
"v_pk_add_f32 v[230:231], v[230:231], v[70:71]\n"
"s_waitcnt lgkmcnt(12)\n"
"v_pk_add_f32 v[232:233], v[232:233], v[72:73]\n"
"v_pk_add_f32 v[234:235], v[234:235], v[74:75]\n"
"s_waitcnt lgkmcnt(11)\n"
"v_pk_add_f32 v[236:237], v[236:237], v[76:77]\n"
"v_pk_add_f32 v[238:239], v[238:239], v[78:79]\n"
"s_waitcnt lgkmcnt(10)\n"
"v_pk_add_f32 v[240:241], v[240:241], v[80:81]\n"
"v_pk_add_f32 v[242:243], v[242:243], v[82:83]\n"
"s_waitcnt lgkmcnt(9)\n"
"v_pk_add_f32 v[224:225], v[224:225], v[84:85]\n"
"v_pk_add_f32 v[226:227], v[226:227], v[86:87]\n"
"s_waitcnt lgkmcnt(8)\n"
"v_pk_add_f32 v[228:229], v[228:229], v[88:89]\n"
"v_pk_add_f32 v[230:231], v[230:231], v[90:91]\n"
"s_waitcnt lgkmcnt(7)\n"
"v_pk_add_f32 v[232:233], v[232:233], v[92:93]\n"
"v_pk_add_f32 v[234:235], v[234:235], v[94:95]\n"
"s_waitcnt lgkmcnt(6)\n"
"v_pk_add_f32 v[236:237], v[236:237], v[96:97]\n"
"v_pk_add_f32 v[238:239], v[238:239], v[98:99]\n"
"s_waitcnt lgkmcnt(5)\n"
"v_pk_add_f32 v[240:241], v[240:241], v[100:101]\n"
"v_pk_add_f32 v[242:243], v[242:243], v[102:103]\n"
"s_waitcnt lgkmcnt(4)\n"
"v_pk_add_f32 v[224:225], v[224:225], v[104:105]\n"
"v_pk_add_f32 v[226:227], v[226:227], v[106:107]\n"
"s_waitcnt lgkmcnt(3)\n"
"v_pk_add_f32 v[228:229], v[228:229], v[108:109]\n"
"v_pk_add_f32 v[230:231], v[230:231], v[110:111]\n"
"s_waitcnt lgkmcnt(2)\n"
"v_pk_add_f32 v[232:233], v[232:233], v[112:113]\n"
"v_pk_add_f32 v[234:235], v[234:235], v[114:115]\n"
"s_waitcnt lgkmcnt(1)\n"
"v_pk_add_f32 v[236:237], v[236:237], v[116:117]\n"
"v_pk_add_f32 v[238:239], v[238:239], v[118:119]\n"
"s_waitcnt lgkmcnt(0)\n"
"v_pk_add_f32 v[240:241], v[240:241], v[120:121]\n"
"v_pk_add_f32 v[242:243], v[242:243], v[122:123]\n"
"v_pk_add_f32 v[124:125], v[124:125], v[224:225]\n"
"v_pk_add_f32 v[126:127], v[126:127], v[226:227]\n"
"v_pk_add_f32 v[128:129], v[128:129], v[228:229]\n"
"v_pk_add_f32 v[130:131], v[130:131], v[230:231]\n"
"v_pk_add_f32 v[132:133], v[132:133], v[232:233]\n"
"v_pk_add_f32 v[134:135], v[134:135], v[234:235]\n"
"v_pk_add_f32 v[136:137], v[136:137], v[236:237]\n"
"v_pk_add_f32 v[138:139], v[138:139], v[238:239]\n"
"v_pk_add_f32 v[140:141], v[140:141], v[240:241]\n"
"v_pk_add_f32 v[142:143], v[142:143], v[242:243]\n"
"v_cvt_pk_bf16_f32 v32, v124, v125\n"
"v_cvt_pk_bf16_f32 v33, v126, v127\n"
"v_cvt_pk_bf16_f32 v34, v128, v129\n"
"v_cvt_pk_bf16_f32 v35, v130, v131\n"
"v_cvt_pk_bf16_f32 v36, v132, v133\n"
"v_cvt_pk_bf16_f32 v37, v134, v135\n"
"v_cvt_pk_bf16_f32 v38, v136, v137\n"
"v_cvt_pk_bf16_f32 v39, v138, v139\n"
"v_cvt_pk_bf16_f32 v40, v140, v141\n"
"v_cvt_pk_bf16_f32 v41, v142, v143\n"
"v_lshlrev_b32 v64, 16, v32\n"
"v_and_b32 v65, 0xffff0000, v32\n"
"v_lshlrev_b32 v66, 16, v33\n"
"v_and_b32 v67, 0xffff0000, v33\n"
"v_lshlrev_b32 v68, 16, v34\n"
"v_and_b32 v69, 0xffff0000, v34\n"
"v_lshlrev_b32 v70, 16, v35\n"
"v_and_b32 v71, 0xffff0000, v35\n"
"v_lshlrev_b32 v72, 16, v36\n"
"v_and_b32 v73, 0xffff0000, v36\n"
"v_lshlrev_b32 v74, 16, v37\n"
"v_and_b32 v75, 0xffff0000, v37\n"
"v_lshlrev_b32 v76, 16, v38\n"
"v_and_b32 v77, 0xffff0000, v38\n"
"v_lshlrev_b32 v78, 16, v39\n"
"v_and_b32 v79, 0xffff0000, v39\n"
"v_lshlrev_b32 v80, 16, v40\n"
"v_and_b32 v81, 0xffff0000, v40\n"
"v_lshlrev_b32 v82, 16, v41\n"
"v_and_b32 v83, 0xffff0000, v41\n"
"v_pk_add_f32 v[84:85], v[64:65], v[68:69]\n"
"v_pk_add_f32 v[88:89], v[72:73], v[76:77]\n"
"v_pk_add_f32 v[86:87], v[66:67], v[70:71]\n"
"v_pk_add_f32 v[90:91], v[74:75], v[78:79]\n"
"v_pk_add_f32 v[84:85], v[84:85], v[80:81]\n"
"v_pk_add_f32 v[86:87], v[86:87], v[82:83]\n"
"v_pk_add_f32 v[84:85], v[84:85], v[88:89]\n"
"v_pk_add_f32 v[86:87], v[86:87], v[90:91]\n"
"s_nop 1\n"
"v_add_f32_dpp v84, v84, v84 row_ror:8 row_mask:0xf bank_mask:0xf\n"
"v_add_f32_dpp v85, v85, v85 row_ror:8 row_mask:0xf bank_mask:0xf\n"
"v_add_f32_dpp v86, v86, v86 row_ror:8 row_mask:0xf bank_mask:0xf\n"
"v_add_f32_dpp v87, v87, v87 row_ror:8 row_mask:0xf bank_mask:0xf\n"
"s_nop 1\n"
"v_add_f32_dpp v84, v84, v84 row_ror:4 row_mask:0xf bank_mask:0xf\n"
"v_add_f32_dpp v85, v85, v85 row_ror:4 row_mask:0xf bank_mask:0xf\n"
"v_add_f32_dpp v86, v86, v86 row_ror:4 row_mask:0xf bank_mask:0xf\n"
"v_add_f32_dpp v87, v87, v87 row_ror:4 row_mask:0xf bank_mask:0xf\n"
"s_nop 1\n"
"v_add_f32_dpp v84, v84, v84 row_ror:2 row_mask:0xf bank_mask:0xf\n"
"v_add_f32_dpp v85, v85, v85 row_ror:2 row_mask:0xf bank_mask:0xf\n"
"v_add_f32_dpp v86, v86, v86 row_ror:2 row_mask:0xf bank_mask:0xf\n"
"v_add_f32_dpp v87, v87, v87 row_ror:2 row_mask:0xf bank_mask:0xf\n"
"s_nop 1\n"
"v_add_f32_dpp v84, v84, v84 row_ror:1 row_mask:0xf bank_mask:0xf\n"
"v_add_f32_dpp v85, v85, v85 row_ror:1 row_mask:0xf bank_mask:0xf\n"
"v_add_f32_dpp v86, v86, v86 row_ror:1 row_mask:0xf bank_mask:0xf\n"
"v_add_f32_dpp v87, v87, v87 row_ror:1 row_mask:0xf bank_mask:0xf\n"
"v_pk_mul_f32 v[84:85], v[84:85], s[98:99]\n"
"v_pk_mul_f32 v[86:87], v[86:87], s[98:99]\n"
"v_pk_add_f32 v[92:93], v[64:65], v[84:85] neg_lo:[0,1] neg_hi:[0,1]\n"
"v_pk_add_f32 v[94:95], v[66:67], v[86:87] neg_lo:[0,1] neg_hi:[0,1]\n"
"v_pk_add_f32 v[96:97], v[68:69], v[84:85] neg_lo:[0,1] neg_hi:[0,1]\n"
"v_pk_add_f32 v[98:99], v[70:71], v[86:87] neg_lo:[0,1] neg_hi:[0,1]\n"
"v_pk_add_f32 v[100:101], v[72:73], v[84:85] neg_lo:[0,1] neg_hi:[0,1]\n"
"v_pk_add_f32 v[102:103], v[74:75], v[86:87] neg_lo:[0,1] neg_hi:[0,1]\n"
"v_pk_add_f32 v[104:105], v[76:77], v[84:85] neg_lo:[0,1] neg_hi:[0,1]\n"
"v_pk_add_f32 v[106:107], v[78:79], v[86:87] neg_lo:[0,1] neg_hi:[0,1]\n"
"v_pk_add_f32 v[108:109], v[80:81], v[84:85] neg_lo:[0,1] neg_hi:[0,1]\n"
"v_pk_add_f32 v[110:111], v[82:83], v[86:87] neg_lo:[0,1] neg_hi:[0,1]\n"
"v_pk_mul_f32 v[88:89], v[92:93], v[92:93]\n"
"v_pk_mul_f32 v[90:91], v[94:95], v[94:95]\n"
"v_pk_fma_f32 v[88:89], v[96:97], v[96:97], v[88:89]\n"
"v_pk_fma_f32 v[90:91], v[98:99], v[98:99], v[90:91]\n"
"v_pk_fma_f32 v[88:89], v[100:101], v[100:101], v[88:89]\n"
"v_pk_fma_f32 v[90:91], v[102:103], v[102:103], v[90:91]\n"
"v_pk_fma_f32 v[88:89], v[104:105], v[104:105], v[88:89]\n"
"v_pk_fma_f32 v[90:91], v[106:107], v[106:107], v[90:91]\n"
"v_pk_fma_f32 v[88:89], v[108:109], v[108:109], v[88:89]\n"
"v_pk_fma_f32 v[90:91], v[110:111], v[110:111], v[90:91]\n"
"s_nop 1\n"
"v_add_f32_dpp v88, v88, v88 row_ror:8 row_mask:0xf bank_mask:0xf\n"
"v_add_f32_dpp v89, v89, v89 row_ror:8 row_mask:0xf bank_mask:0xf\n"
"v_add_f32_dpp v90, v90, v90 row_ror:8 row_mask:0xf bank_mask:0xf\n"
"v_add_f32_dpp v91, v91, v91 row_ror:8 row_mask:0xf bank_mask:0xf\n"
"s_nop 1\n"
"v_add_f32_dpp v88, v88, v88 row_ror:4 row_mask:0xf bank_mask:0xf\n"
"v_add_f32_dpp v89, v89, v89 row_ror:4 row_mask:0xf bank_mask:0xf\n"
"v_add_f32_dpp v90, v90, v90 row_ror:4 row_mask:0xf bank_mask:0xf\n"
"v_add_f32_dpp v91, v91, v91 row_ror:4 row_mask:0xf bank_mask:0xf\n"
"s_nop 1\n"
"v_add_f32_dpp v88, v88, v88 row_ror:2 row_mask:0xf bank_mask:0xf\n"
"v_add_f32_dpp v89, v89, v89 row_ror:2 row_mask:0xf bank_mask:0xf\n"
"v_add_f32_dpp v90, v90, v90 row_ror:2 row_mask:0xf bank_mask:0xf\n"
"v_add_f32_dpp v91, v91, v91 row_ror:2 row_mask:0xf bank_mask:0xf\n"
"s_nop 1\n"
"v_add_f32_dpp v88, v88, v88 row_ror:1 row_mask:0xf bank_mask:0xf\n"
"v_add_f32_dpp v89, v89, v89 row_ror:1 row_mask:0xf bank_mask:0xf\n"
"v_add_f32_dpp v90, v90, v90 row_ror:1 row_mask:0xf bank_mask:0xf\n"
"v_add_f32_dpp v91, v91, v91 row_ror:1 row_mask:0xf bank_mask:0xf\n"
"v_cndmask_b32 v92, v84, v88, s[90:91]\n"
"v_cndmask_b32 v93, v85, v89, s[90:91]\n"
"v_cndmask_b32 v94, v86, v90, s[90:91]\n"
"v_cndmask_b32 v95, v87, v91, s[90:91]\n"
"v_cndmask_b32 v92, v92, v93, s[86:87]\n"
"v_cndmask_b32 v94, v94, v95, s[86:87]\n"
"v_cndmask_b32 v92, v92, v94, s[88:89]\n"
"v_mov_b32 v93, 1\n"
"s_lshl_b32 s72, s77, 10\n"
"s_add_u32 s72, s72, s81\n"
"s_mov_b64 exec, s[92:93]\n"
"buffer_store_dwordx2 v[92:93], v44, s[40:43], s72 offen sc1\n"
"s_mov_b64 exec, -1\n"
"s_add_u32 m0, s64, 16384\n"
"s_nop 0\n"
"buffer_load_dwordx4 v16, s[44:47], s65 offen lds\n"
"buffer_load_dwordx4 v17, s[44:47], s65 offen offset:1024 lds\n"
"buffer_load_dwordx4 v18, s[44:47], s65 offen offset:2048 lds\n"
"buffer_load_dwordx4 v19, s[44:47], s65 offen offset:3072 lds\n"
"s_add_u32 m0, s64, 20480\n"
"s_nop 0\n"
"buffer_load_dwordx4 v16, s[48:51], s65 offen lds\n"
"buffer_load_dwordx4 v17, s[48:51], s65 offen offset:1024 lds\n"
"buffer_load_dwordx4 v18, s[48:51], s65 offen offset:2048 lds\n"
"buffer_load_dwordx4 v19, s[48:51], s65 offen offset:3072 lds\n"
"s_add_u32 s65, s65, 256\n"
"s_and_b32 s65, s65, 0xfff\n"
"s_waitcnt vmcnt(17)\n"
"ds_read_b128 a[0:3], v25\n"
"ds_read_b128 a[4:7], v25 offset:4096\n"
"ds_read_b128 a[8:11], v26\n"
"ds_read_b128 a[12:15], v26 offset:4096\n"
"ds_read_b128 a[16:19], v27\n"
"ds_read_b128 a[20:23], v27 offset:4096\n"
"ds_read_b128 a[24:27], v28\n"
"ds_read_b128 a[28:31], v28 offset:4096\n"
"s_waitcnt lgkmcnt(0)\n"
"s_add_u32 m0, s64, 0\n"
"s_nop 0\n"
"buffer_load_dwordx4 v16, s[44:47], s65 offen lds\n"
"buffer_load_dwordx4 v17, s[44:47], s65 offen offset:1024 lds\n"
"buffer_load_dwordx4 v18, s[44:47], s65 offen offset:2048 lds\n"
"buffer_load_dwordx4 v19, s[44:47], s65 offen offset:3072 lds\n"
"s_add_u32 m0, s64, 4096\n"
"s_nop 0\n"
"buffer_load_dwordx4 v16, s[48:51], s65 offen lds\n"
"buffer_load_dwordx4 v17, s[48:51], s65 offen offset:1024 lds\n"
"buffer_load_dwordx4 v18, s[48:51], s65 offen offset:2048 lds\n"
"buffer_load_dwordx4 v19, s[48:51], s65 offen offset:3072 lds\n"
"s_add_u32 s65, s65, 256\n"
"s_and_b32 s65, s65, 0xfff\n"
"s_waitcnt vmcnt(17)\n"
"ds_read_b128 a[32:35], v25 offset:8192\n"
"ds_read_b128 a[36:39], v25 offset:12288\n"
"ds_read_b128 a[40:43], v26 offset:8192\n"
"ds_read_b128 a[44:47], v26 offset:12288\n"
"ds_read_b128 a[48:51], v27 offset:8192\n"
"ds_read_b128 a[52:55], v27 offset:12288\n"
"ds_read_b128 a[56:59], v28 offset:8192\n"
"ds_read_b128 a[60:63], v28 offset:12288\n"
"s_waitcnt lgkmcnt(0)\n"
"s_add_u32 m0, s64, 8192\n"
"s_nop 0\n"
"buffer_load_dwordx4 v16, s[44:47], s65 offen lds\n"
"buffer_load_dwordx4 v17, s[44:47], s65 offen offset:1024 lds\n"
"buffer_load_dwordx4 v18, s[44:47], s65 offen offset:2048 lds\n"
"buffer_load_dwordx4 v19, s[44:47], s65 offen offset:3072 lds\n"
"s_add_u32 m0, s64, 12288\n"
"s_nop 0\n"
"buffer_load_dwordx4 v16, s[48:51], s65 offen lds\n"
"buffer_load_dwordx4 v17, s[48:51], s65 offen offset:1024 lds\n"
"buffer_load_dwordx4 v18, s[48:51], s65 offen offset:2048 lds\n"
"buffer_load_dwordx4 v19, s[48:51], s65 offen offset:3072 lds\n"
"s_add_u32 s65, s65, 256\n"
"s_and_b32 s65, s65, 0xfff\n"
"s_cmp_lg_u32 s84, 0\n"
"s_cbranch_scc1 49f\n"
"s_mov_b32 s70, 65536\n"
"buffer_load_dwordx4 v[144:147], v45, s[40:43], s81 offen sc1\n"
"buffer_load_dwordx4 v[148:151], v45, s[40:43], s81 offen offset:1024 sc1\n"
"41:\n"
"s_waitcnt vmcnt(0)\n"
"v_and_b32 v52, v145, v147\n"
"v_and_b32 v53, v149, v151\n"
"v_and_b32 v52, v52, v53\n"
"v_or_b32 v53, v145, v147\n"
"v_or3_b32 v53, v53, v149, v151\n"
"v_cmp_ne_u32 vcc, s85, v52\n"
"v_cmp_ne_u32 s[94:95], s85, v53\n"
"v_mov_b32 v112, v144\n"
"v_mov_b32 v114, v146\n"
"v_mov_b32 v116, v148\n"
"v_mov_b32 v118, v150\n"
"s_or_b64 vcc, vcc, s[94:95]\n"
"s_cbranch_vccz 49f\n"
"buffer_load_dwordx4 v[144:147], v45, s[40:43], s81 offen sc1\n"
"buffer_load_dwordx4 v[148:151], v45, s[40:43], s81 offen offset:1024 sc1\n"
"s_sub_u32 s70, s70, 1\n"
"s_cmp_lg_u32 s70, 0\n"
"s_cbranch_scc1 41b\n"
"s_mov_b32 s84, 1\n"
"49:\n"
"v_add_f32 v120, v112, v116\n"
"v_mov_b32 v122, v120\n"
"s_nop 1\n"
"v_permlane16_swap_b32 v120, v122\n"
"s_nop 1\n"
"v_add_f32 v120, v120, v122\n"
"v_mov_b32 v122, v120\n"
"s_nop 1\n"
"v_permlane32_swap_b32 v120, v122\n"
"s_nop 1\n"
"v_add_f32 v120, v120, v122\n"
"v_mul_f32 v120, 0x3e000000, v120\n"
"v_sub_f32 v122, v112, v120\n"
"v_sub_f32 v123, v116, v120\n"
"v_mul_f32 v122, v122, v122\n"
"v_fmac_f32 v122, v123, v123\n"
"v_add_f32 v121, v114, v118\n"
"v_mov_b32 v123, 0x42a00000\n"
"v_fmac_f32 v121, v123, v122\n"
"v_mov_b32 v122, v121\n"
"s_nop 1\n"
"v_permlane16_swap_b32 v121, v122\n"
"s_nop 1\n"
"v_add_f32 v121, v121, v122\n"
"v_mov_b32 v122, v121\n"
"s_nop 1\n"
"v_permlane32_swap_b32 v121, v122\n"
"s_nop 1\n"
"v_add_f32 v121, v121, v122\n"
"v_mov_b32 v122, 0x3727c5ac\n"
"v_mov_b32 v123, 0x3acccccd\n"
"v_fma_f32 v121, v121, v123, v122\n"
"v_rsq_f32 v121, v121\n"
"s_nop 1\n"
"v_add_u32 v52, 0, v46\n"
"ds_bpermute_b32 v124, v52, v120\n"
"ds_bpermute_b32 v128, v52, v121\n"
"v_add_u32 v52, 4, v46\n"
"ds_bpermute_b32 v125, v52, v120\n"
"ds_bpermute_b32 v129, v52, v121\n"
"v_add_u32 v52, 8, v46\n"
"ds_bpermute_b32 v126, v52, v120\n"
"ds_bpermute_b32 v130, v52, v121\n"
"v_add_u32 v52, 12, v46\n"
"ds_bpermute_b32 v127, v52, v120\n"
"ds_bpermute_b32 v131, v52, v121\n"
"s_waitcnt lgkmcnt(0)\n"
"v_pk_add_f32 v[64:65], v[64:65], v[124:125] neg_lo:[0,1] neg_hi:[0,1]\n"
"v_pk_add_f32 v[66:67], v[66:67], v[126:127] neg_lo:[0,1] neg_hi:[0,1]\n"
"v_pk_mul_f32 v[64:65], v[64:65], v[128:129]\n"
"v_pk_mul_f32 v[66:67], v[66:67], v[130:131]\n"
"v_cvt_pk_bf16_f32 v132, v64, v65\n"
"v_cvt_pk_bf16_f32 v133, v66, v67\n"
"v_pk_add_f32 v[68:69], v[68:69], v[124:125] neg_lo:[0,1] neg_hi:[0,1]\n"
"v_pk_add_f32 v[70:71], v[70:71], v[126:127] neg_lo:[0,1] neg_hi:[0,1]\n"
"v_pk_mul_f32 v[68:69], v[68:69], v[128:129]\n"
"v_pk_mul_f32 v[70:71], v[70:71], v[130:131]\n"
"v_cvt_pk_bf16_f32 v134, v68, v69\n"
"v_cvt_pk_bf16_f32 v135, v70, v71\n"
"v_pk_add_f32 v[72:73], v[72:73], v[124:125] neg_lo:[0,1] neg_hi:[0,1]\n"
"v_pk_add_f32 v[74:75], v[74:75], v[126:127] neg_lo:[0,1] neg_hi:[0,1]\n"
"v_pk_mul_f32 v[72:73], v[72:73], v[128:129]\n"
"v_pk_mul_f32 v[74:75], v[74:75], v[130:131]\n"
"v_cvt_pk_bf16_f32 v136, v72, v73\n"
"v_cvt_pk_bf16_f32 v137, v74, v75\n"
"v_pk_add_f32 v[76:77], v[76:77], v[124:125] neg_lo:[0,1] neg_hi:[0,1]\n"
"v_pk_add_f32 v[78:79], v[78:79], v[126:127] neg_lo:[0,1] neg_hi:[0,1]\n"
"v_pk_mul_f32 v[76:77], v[76:77], v[128:129]\n"
"v_pk_mul_f32 v[78:79], v[78:79], v[130:131]\n"
"v_cvt_pk_bf16_f32 v138, v76, v77\n"
"v_cvt_pk_bf16_f32 v139, v78, v79\n"
"v_pk_add_f32 v[80:81], v[80:81], v[124:125] neg_lo:[0,1] neg_hi:[0,1]\n"
"v_pk_add_f32 v[82:83], v[82:83], v[126:127] neg_lo:[0,1] neg_hi:[0,1]\n"
"v_pk_mul_f32 v[80:81], v[80:81], v[128:129]\n"
"v_pk_mul_f32 v[82:83], v[82:83], v[130:131]\n"
"v_cvt_pk_bf16_f32 v140, v80, v81\n"
"v_cvt_pk_bf16_f32 v141, v82, v83\n"
"s_nop 1\n"
"v_permlane16_swap_b32 v132, v134\n"
"v_permlane16_swap_b32 v133, v135\n"
"v_permlane16_swap_b32 v136, v138\n"
"v_permlane16_swap_b32 v137, v139\n"
"s_mul_i32 s73, s76, 10240\n"
"s_add_u32 s72, s68, s73\n"
"s_add_u32 s72, s72, 2621440\n"
"buffer_store_dwordx4 v[132:135], v43, s[40:43], s72 offen sc1\n"
"buffer_store_dwordx4 v[136:139], v43, s[40:43], s72 offen offset:512 sc1\n"
"buffer_store_dwordx2 v[140:141], v42, s[40:43], s72 offen offset:1024 sc1\n"
"s_mul_i32 s72, s76, 4\n"
"s_add_u32 s72, s72, s83\n"
"s_waitcnt vmcnt(0)\n"
"s_barrier\n"
"s_cmp_lg_u32 s78, 0\n"
"s_cbranch_scc1 8f\n"
"s_mov_b64 exec, 1\n"
"v_mov_b32 v52, 1\n"
"v_mov_b32 v53, 0\n"
"buffer_store_dword v52, v53, s[40:43], s72 offen sc1\n"
"s_mov_b64 exec, -1\n"
"8:\n"
"s_waitcnt vmcnt(16)\n"
"ds_read_b128 a[64:67], v25 offset:16384\n"
"ds_read_b128 a[68:71], v25 offset:20480\n"
"ds_read_b128 a[72:75], v26 offset:16384\n"
"ds_read_b128 a[76:79], v26 offset:20480\n"
"ds_read_b128 a[80:83], v27 offset:16384\n"
"ds_read_b128 a[84:87], v27 offset:20480\n"
"ds_read_b128 a[88:91], v28 offset:16384\n"
"ds_read_b128 a[92:95], v28 offset:20480\n"
"s_waitcnt lgkmcnt(0)\n"
"s_add_u32 m0, s64, 16384\n"
"s_nop 0\n"
"buffer_load_dwordx4 v16, s[44:47], s65 offen lds\n"
"buffer_load_dwordx4 v17, s[44:47], s65 offen offset:1024 lds\n"
"buffer_load_dwordx4 v18, s[44:47], s65 offen offset:2048 lds\n"
"buffer_load_dwordx4 v19, s[44:47], s65 offen offset:3072 lds\n"
"s_add_u32 m0, s64, 20480\n"
"s_nop 0\n"
"buffer_load_dwordx4 v16, s[48:51], s65 offen lds\n"
"buffer_load_dwordx4 v17, s[48:51], s65 offen offset:1024 lds\n"
"buffer_load_dwordx4 v18, s[48:51], s65 offen offset:2048 lds\n"
"buffer_load_dwordx4 v19, s[48:51], s65 offen offset:3072 lds\n"
"s_add_u32 s65, s65, 256\n"
"s_and_b32 s65, s65, 0xfff\n"
"s_waitcnt vmcnt(16)\n"
"ds_read_b128 a[96:99], v25\n"
"ds_read_b128 a[100:103], v25 offset:4096\n"
"ds_read_b128 a[104:107], v26\n"
"ds_read_b128 a[108:111], v26 offset:4096\n"
"ds_read_b128 a[112:115], v27\n"
"ds_read_b128 a[116:119], v27 offset:4096\n"
"ds_read_b128 a[120:123], v28\n"
"ds_read_b128 a[124:127], v28 offset:4096\n"
"s_waitcnt lgkmcnt(0)\n"
"s_add_u32 m0, s64, 0\n"
"s_nop 0\n"
"buffer_load_dwordx4 v16, s[44:47], s65 offen lds\n"
"buffer_load_dwordx4 v17, s[44:47], s65 offen offset:1024 lds\n"
"buffer_load_dwordx4 v18, s[44:47], s65 offen offset:2048 lds\n"
"buffer_load_dwordx4 v19, s[44:47], s65 offen offset:3072 lds\n"
"s_add_u32 m0, s64, 4096\n"
"s_nop 0\n"
"buffer_load_dwordx4 v16, s[48:51], s65 offen lds\n"
"buffer_load_dwordx4 v17, s[48:51], s65 offen offset:1024 lds\n"
"buffer_load_dwordx4 v18, s[48:51], s65 offen offset:2048 lds\n"
"buffer_load_dwordx4 v19, s[48:51], s65 offen offset:3072 lds\n"
"s_add_u32 s65, s65, 256\n"
"s_and_b32 s65, s65, 0xfff\n"
"s_waitcnt vmcnt(16)\n"
"ds_read_b128 a[128:131], v25 offset:8192\n"
"ds_read_b128 a[132:135], v25 offset:12288\n"
"ds_read_b128 a[136:139], v26 offset:8192\n"
"ds_read_b128 a[140:143], v26 offset:12288\n"
"ds_read_b128 a[144:147], v27 offset:8192\n"
"ds_read_b128 a[148:151], v27 offset:12288\n"
"ds_read_b128 a[152:155], v28 offset:8192\n"
"ds_read_b128 a[156:159], v28 offset:12288\n"
"s_waitcnt lgkmcnt(0)\n"
"s_add_u32 m0, s64, 8192\n"
"s_nop 0\n"
"buffer_load_dwordx4 v16, s[44:47], s65 offen lds\n"
"buffer_load_dwordx4 v17, s[44:47], s65 offen offset:1024 lds\n"
"buffer_load_dwordx4 v18, s[44:47], s65 offen offset:2048 lds\n"
"buffer_load_dwordx4 v19, s[44:47], s65 offen offset:3072 lds\n"
"s_add_u32 m0, s64, 12288\n"
"s_nop 0\n"
"buffer_load_dwordx4 v16, s[48:51], s65 offen lds\n"
"buffer_load_dwordx4 v17, s[48:51], s65 offen offset:1024 lds\n"
"buffer_load_dwordx4 v18, s[48:51], s65 offen offset:2048 lds\n"
"buffer_load_dwordx4 v19, s[48:51], s65 offen offset:3072 lds\n"
"s_add_u32 s65, s65, 256\n"
"s_and_b32 s65, s65, 0xfff\n"
"s_waitcnt vmcnt(16)\n"
"ds_read_b128 a[160:163], v25 offset:16384\n"
"ds_read_b128 a[164:167], v25 offset:20480\n"
"ds_read_b128 a[168:171], v26 offset:16384\n"
"ds_read_b128 a[172:175], v26 offset:20480\n"
"ds_read_b128 a[176:179], v27 offset:16384\n"
"ds_read_b128 a[180:183], v27 offset:20480\n"
"ds_read_b128 a[184:187], v28 offset:16384\n"
"ds_read_b128 a[188:191], v28 offset:20480\n"
"s_waitcnt lgkmcnt(0)\n"
"s_add_u32 m0, s64, 16384\n"
"s_nop 0\n"
"buffer_load_dwordx4 v16, s[44:47], s65 offen lds\n"
"buffer_load_dwordx4 v17, s[44:47], s65 offen offset:1024 lds\n"
"buffer_load_dwordx4 v18, s[44:47], s65 offen offset:2048 lds\n"
"buffer_load_dwordx4 v19, s[44:47], s65 offen offset:3072 lds\n"
"s_add_u32 m0, s64, 20480\n"
"s_nop 0\n"
"buffer_load_dwordx4 v16, s[48:51], s65 offen lds\n"
"buffer_load_dwordx4 v17, s[48:51], s65 offen offset:1024 lds\n"
"buffer_load_dwordx4 v18, s[48:51], s65 offen offset:2048 lds\n"
"buffer_load_dwordx4 v19, s[48:51], s65 offen offset:3072 lds\n"
"s_add_u32 s65, s65, 256\n"
"s_and_b32 s65, s65, 0xfff\n"
"s_cmp_lg_u32 s78, 0\n"
"s_cbranch_scc1 68f\n"
"s_cmp_lg_u32 s84, 0\n"
"s_cbranch_scc1 69f\n"
"s_mov_b32 s70, 65536\n"
"buffer_load_dword v56, v49, s[40:43], s83 offen sc1\n"
"61:\n"
"s_waitcnt vmcnt(0)\n"
"v_cmp_ne_u32 vcc, s85, v56\n"
"s_nop 4\n"
"s_cbranch_vccz 69f\n"
"buffer_load_dword v56, v49, s[40:43], s83 offen sc1\n"
"s_sub_u32 s70, s70, 1\n"
"s_cmp_lg_u32 s70, 0\n"
"s_cbranch_scc1 61b\n"
"s_mov_b32 s84, 1\n"
"69:\n"
"68:\n"
"s_barrier\n"
"s_add_u32 s71, s71, 1\n"
"s_branch 2b\n"
"5:\n"
"s_waitcnt vmcnt(0)\n"
"s_waitcnt lgkmcnt(0)\n"
"s_barrier\n"
"s_add_u32 s72, s78, 1\n"
"s_and_b32 s72, s72, 3\n"
"s_mul_i32 s72, s72, 15360\n"
"s_add_u32 s72, s72, 108544\n"
"s_add_u32 s72, s72, s96\n"
"v_add_u32 v53, s72, v30\n"
"s_add_u32 s72, s78, 2\n"
"s_and_b32 s72, s72, 3\n"
"s_mul_i32 s72, s72, 15360\n"
"s_add_u32 s72, s72, 103424\n"
"s_add_u32 s72, s72, s96\n"
"v_add_u32 v54, s72, v30\n"
"s_add_u32 s72, s78, 3\n"
"s_and_b32 s72, s72, 3\n"
"s_mul_i32 s72, s72, 15360\n"
"s_add_u32 s72, s72, 98304\n"
"s_add_u32 s72, s72, s96\n"
"v_add_u32 v55, s72, v30\n"
"ds_read_b128 v[64:67], v53\n"
"ds_read_b128 v[68:71], v53 offset:1024\n"
"ds_read_b128 v[72:75], v53 offset:2048\n"
"ds_read_b128 v[76:79], v53 offset:3072\n"
"ds_read_b128 v[80:83], v53 offset:4096\n"
"ds_read_b128 v[84:87], v54\n"
"ds_read_b128 v[88:91], v54 offset:1024\n"
"ds_read_b128 v[92:95], v54 offset:2048\n"
"ds_read_b128 v[96:99], v54 offset:3072\n"
"ds_read_b128 v[100:103], v54 offset:4096\n"
"ds_read_b128 v[104:107], v55\n"
"ds_read_b128 v[108:111], v55 offset:1024\n"
"ds_read_b128 v[112:115], v55 offset:2048\n"
"ds_read_b128 v[116:119], v55 offset:3072\n"
"ds_read_b128 v[120:123], v55 offset:4096\n"
"v_lshlrev_b32 v124, 16, v32\n"
"v_and_b32 v125, 0xffff0000, v32\n"
"v_lshlrev_b32 v126, 16, v33\n"
"v_and_b32 v127, 0xffff0000, v33\n"
"v_lshlrev_b32 v128, 16, v34\n"
"v_and_b32 v129, 0xffff0000, v34\n"
"v_lshlrev_b32 v130, 16, v35\n"
"v_and_b32 v131, 0xffff0000, v35\n"
"v_lshlrev_b32 v132, 16, v36\n"
"v_and_b32 v133, 0xffff0000, v36\n"
"v_lshlrev_b32 v134, 16, v37\n"
"v_and_b32 v135, 0xffff0000, v37\n"
"v_lshlrev_b32 v136, 16, v38\n"
"v_and_b32 v137, 0xffff0000, v38\n"
"v_lshlrev_b32 v138, 16, v39\n"
"v_and_b32 v139, 0xffff0000, v39\n"
"v_lshlrev_b32 v140, 16, v40\n"
"v_and_b32 v141, 0xffff0000, v40\n"
"v_lshlrev_b32 v142, 16, v41\n"
"v_and_b32 v143, 0xffff0000, v41\n"
"s_nop 7\n"
"s_waitcnt lgkmcnt(14)\n"
"v_pk_add_f32 v[224:225], v[224:225], v[64:65]\n"
"v_pk_add_f32 v[226:227], v[226:227], v[66:67]\n"
"s_waitcnt lgkmcnt(13)\n"
"v_pk_add_f32 v[228:229], v[228:229], v[68:69]\n"
"v_pk_add_f32 v[230:231], v[230:231], v[70:71]\n"
"s_waitcnt lgkmcnt(12)\n"
"v_pk_add_f32 v[232:233], v[232:233], v[72:73]\n"
"v_pk_add_f32 v[234:235], v[234:235], v[74:75]\n"
"s_waitcnt lgkmcnt(11)\n"
"v_pk_add_f32 v[236:237], v[236:237], v[76:77]\n"
"v_pk_add_f32 v[238:239], v[238:239], v[78:79]\n"
"s_waitcnt lgkmcnt(10)\n"
"v_pk_add_f32 v[240:241], v[240:241], v[80:81]\n"
"v_pk_add_f32 v[242:243], v[242:243], v[82:83]\n"
"s_waitcnt lgkmcnt(9)\n"
"v_pk_add_f32 v[224:225], v[224:225], v[84:85]\n"
"v_pk_add_f32 v[226:227], v[226:227], v[86:87]\n"
"s_waitcnt lgkmcnt(8)\n"
"v_pk_add_f32 v[228:229], v[228:229], v[88:89]\n"
"v_pk_add_f32 v[230:231], v[230:231], v[90:91]\n"
"s_waitcnt lgkmcnt(7)\n"
"v_pk_add_f32 v[232:233], v[232:233], v[92:93]\n"
"v_pk_add_f32 v[234:235], v[234:235], v[94:95]\n"
"s_waitcnt lgkmcnt(6)\n"
"v_pk_add_f32 v[236:237], v[236:237], v[96:97]\n"
"v_pk_add_f32 v[238:239], v[238:239], v[98:99]\n"
"s_waitcnt lgkmcnt(5)\n"
"v_pk_add_f32 v[240:241], v[240:241], v[100:101]\n"
"v_pk_add_f32 v[242:243], v[242:243], v[102:103]\n"
"s_waitcnt lgkmcnt(4)\n"
"v_pk_add_f32 v[224:225], v[224:225], v[104:105]\n"
"v_pk_add_f32 v[226:227], v[226:227], v[106:107]\n"
"s_waitcnt lgkmcnt(3)\n"
"v_pk_add_f32 v[228:229], v[228:229], v[108:109]\n"
"v_pk_add_f32 v[230:231], v[230:231], v[110:111]\n"
"s_waitcnt lgkmcnt(2)\n"
"v_pk_add_f32 v[232:233], v[232:233], v[112:113]\n"
"v_pk_add_f32 v[234:235], v[234:235], v[114:115]\n"
"s_waitcnt lgkmcnt(1)\n"
"v_pk_add_f32 v[236:237], v[236:237], v[116:117]\n"
"v_pk_add_f32 v[238:239], v[238:239], v[118:119]\n"
"s_waitcnt lgkmcnt(0)\n"
"v_pk_add_f32 v[240:241], v[240:241], v[120:121]\n"
"v_pk_add_f32 v[242:243], v[242:243], v[122:123]\n"
"v_pk_add_f32 v[124:125], v[124:125], v[224:225]\n"
"v_pk_add_f32 v[126:127], v[126:127], v[226:227]\n"
"v_pk_add_f32 v[128:129], v[128:129], v[228:229]\n"
"v_pk_add_f32 v[130:131], v[130:131], v[230:231]\n"
"v_pk_add_f32 v[132:133], v[132:133], v[232:233]\n"
"v_pk_add_f32 v[134:135], v[134:135], v[234:235]\n"
"v_pk_add_f32 v[136:137], v[136:137], v[236:237]\n"
"v_pk_add_f32 v[138:139], v[138:139], v[238:239]\n"
"v_pk_add_f32 v[140:141], v[140:141], v[240:241]\n"
"v_pk_add_f32 v[142:143], v[142:143], v[242:243]\n"
"v_cvt_pk_bf16_f32 v32, v124, v125\n"
"v_cvt_pk_bf16_f32 v33, v126, v127\n"
"v_cvt_pk_bf16_f32 v34, v128, v129\n"
"v_cvt_pk_bf16_f32 v35, v130, v131\n"
"v_cvt_pk_bf16_f32 v36, v132, v133\n"
"v_cvt_pk_bf16_f32 v37, v134, v135\n"
"v_cvt_pk_bf16_f32 v38, v136, v137\n"
"v_cvt_pk_bf16_f32 v39, v138, v139\n"
"v_cvt_pk_bf16_f32 v40, v140, v141\n"
"v_cvt_pk_bf16_f32 v41, v142, v143\n"
"v_lshrrev_b32 v52, 4, v30\n"
"v_lshrrev_b32 v53, 4, v52\n"
"v_and_b32 v64, 15, v52\n"
"v_lshlrev_b32 v53, 2, v53\n"
"s_lshl_b32 s72, s78, 4\n"
"v_add_u32 v53, s72, v53\n"
"v_mul_u32_u24 v53, 176, v53\n"
"v_lshlrev_b32 v64, 1, v64\n"
"v_add_u32 v53, v53, v64\n"
"v_add_u32 v53, s96, v53\n"
"ds_write_b16 v53, v32 offset:0\n"
"ds_write_b16_d16_hi v53, v32 offset:176\n"
"ds_write_b16 v53, v33 offset:352\n"
"ds_write_b16_d16_hi v53, v33 offset:528\n"
"ds_write_b16 v53, v34 offset:32\n"
"ds_write_b16_d16_hi v53, v34 offset:208\n"
"ds_write_b16 v53, v35 offset:384\n"
"ds_write_b16_d16_hi v53, v35 offset:560\n"
"ds_write_b16 v53, v36 offset:64\n"
"ds_write_b16_d16_hi v53, v36 offset:240\n"
"ds_write_b16 v53, v37 offset:416\n"
"ds_write_b16_d16_hi v53, v37 offset:592\n"
"ds_write_b16 v53, v38 offset:96\n"
"ds_write_b16_d16_hi v53, v38 offset:272\n"
"ds_write_b16 v53, v39 offset:448\n"
"ds_write_b16_d16_hi v53, v39 offset:624\n"
"ds_write_b16 v53, v40 offset:128\n"
"ds_write_b16_d16_hi v53, v40 offset:304\n"
"ds_write_b16 v53, v41 offset:480\n"
"ds_write_b16_d16_hi v53, v41 offset:656\n"
"s_waitcnt lgkmcnt(0)\n"
"s_barrier\n"
"s_lshl_b32 s72, s78, 6\n"
"v_add_u32 v64, s72, v52\n"
"s_lshl_b32 s73, s76, 6\n"
"s_mul_i32 s73, s73, 1280\n"
"s_mul_i32 s74, s77, 160\n"
"s_add_u32 s73, s73, s74\n"
"v_add_u32 v66, 0, v64\n"
"v_mul_u32_u24 v67, 6554, v66\n"
"v_lshrrev_b32 v67, 16, v67\n"
"v_mul_u32_u24 v68, 10, v67\n"
"v_sub_u32 v68, v66, v68\n"
"v_lshlrev_b32 v68, 4, v68\n"
"v_mul_u32_u24 v69, 176, v67\n"
"v_add_u32 v69, v69, v68\n"
"v_add_u32 v69, s96, v69\n"
"v_mul_u32_u24 v67, 1280, v67\n"
"v_add_u32 v67, v67, v68\n"
"ds_read_b128 v[70:73], v69\n"
"s_waitcnt lgkmcnt(0)\n"
"buffer_store_dwordx4 v[70:73], v67, s[52:55], s73 offen\n"
"v_add_u32 v74, 256, v64\n"
"v_mul_u32_u24 v75, 6554, v74\n"
"v_lshrrev_b32 v75, 16, v75\n"
"v_mul_u32_u24 v76, 10, v75\n"
"v_sub_u32 v76, v74, v76\n"
"v_lshlrev_b32 v76, 4, v76\n"
"v_mul_u32_u24 v77, 176, v75\n"
"v_add_u32 v77, v77, v76\n"
"v_add_u32 v77, s96, v77\n"
"v_mul_u32_u24 v75, 1280, v75\n"
"v_add_u32 v75, v75, v76\n"
"ds_read_b128 v[78:81], v77\n"
"s_waitcnt lgkmcnt(0)\n"
"buffer_store_dwordx4 v[78:81], v75, s[52:55], s73 offen\n"
"v_add_u32 v82, 512, v64\n"
"v_mul_u32_u24 v83, 6554, v82\n"
"v_lshrrev_b32 v83, 16, v83\n"
"v_mul_u32_u24 v84, 10, v83\n"
"v_sub_u32 v84, v82, v84\n"
"v_lshlrev_b32 v84, 4, v84\n"
"v_mul_u32_u24 v85, 176, v83\n"
"v_add_u32 v85, v85, v84\n"
"v_add_u32 v85, s96, v85\n"
"v_mul_u32_u24 v83, 1280, v83\n"
"v_add_u32 v83, v83, v84\n"
"v_cmp_gt_u32 vcc, 640, v82\n"
"s_nop 4\n"
"s_mov_b64 exec, vcc\n"
"ds_read_b128 v[86:89], v85\n"
"s_waitcnt lgkmcnt(0)\n"
"buffer_store_dwordx4 v[86:89], v83, s[52:55], s73 offen\n"
"s_mov_b64 exec, -1\n"
"s_or_b32 s72, s76, s77\n"
"s_or_b32 s72, s72, s78\n"
"s_cmp_lg_u32 s72, 0\n"
"s_cbranch_scc1 6f\n"
"s_mov_b64 exec, 1\n"
"v_mov_b32 v52, s36\n"
"v_mov_b32 v53, 0\n"
"s_mov_b32 s72, 35783680\n"
"buffer_store_dword v52, v53, s[40:43], s72 offen sc1\n"
"s_mov_b64 exec, -1\n"
"6:\n"
"s_waitcnt vmcnt(0)\n"
        :: "s"(ka), "s"(g), "s"(r), "s"(w), "s"(lbase), "v"(lane)
        : "memory", "scc", "vcc", "v16","v17","v18","v19","v20","v21","v22","v23","v24","v25","v26","v27","v28","v29","v30","v31","v32","v33","v34","v35","v36","v37","v38","v39","v40","v41","v42","v43","v44","v45","v46","v47","v48","v49","v50","v51","v52","v53","v54","v55","v56","v57","v58","v59","v60","v61","v62","v63","v64","v65","v66","v67","v68","v69","v70","v71","v72","v73","v74","v75","v76","v77","v78","v79","v80","v81","v82","v83","v84","v85","v86","v87","v88","v89","v90","v91","v92","v93","v94","v95","v96","v97","v98","v99","v100","v101","v102","v103","v104","v105","v106","v107","v108","v109","v110","v111","v112","v113","v114","v115","v116","v117","v118","v119","v120","v121","v122","v123","v124","v125","v126","v127","v128","v129","v130","v131","v132","v133","v134","v135","v136","v137","v138","v139","v140","v141","v142","v143","v144","v145","v146","v147","v148","v149","v150","v151","v152","v153","v154","v155","v156","v157","v158","v159","v160","v161","v162","v163","v164","v165","v166","v167","v168","v169","v170","v171","v172","v173","v174","v175","v176","v177","v178","v179","v180","v181","v182","v183","v184","v185","v186","v187","v188","v189","v190","v191","v192","v193","v194","v195","v196","v197","v198","v199","v200","v201","v202","v203","v204","v205","v206","v207","v208","v209","v210","v211","v212","v213","v214","v215","v216","v217","v218","v219","v220","v221","v222","v223","v224","v225","v226","v227","v228","v229","v230","v231","v232","v233","v234","v235","v236","v237","v238","v239","v240","v241","v242","v243","v244","v245","v246","v247","v248","v249","v250","v251","v252","v253","v254","v255","a0","a1","a2","a3","a4","a5","a6","a7","a8","a9","a10","a11","a12","a13","a14","a15","a16","a17","a18","a19","a20","a21","a22","a23","a24","a25","a26","a27","a28","a29","a30","a31","a32","a33","a34","a35","a36","a37","a38","a39","a40","a41","a42","a43","a44","a45","a46","a47","a48","a49","a50","a51","a52","a53","a54","a55","a56","a57","a58","a59","a60","a61","a62","a63","a64","a65","a66","a67","a68","a69","a70","a71","a72","a73","a74","a75","a76","a77","a78","a79","a80","a81","a82","a83","a84","a85","a86","a87","a88","a89","a90","a91","a92","a93","a94","a95","a96","a97","a98","a99","a100","a101","a102","a103","a104","a105","a106","a107","a108","a109","a110","a111","a112","a113","a114","a115","a116","a117","a118","a119","a120","a121","a122","a123","a124","a125","a126","a127","a128","a129","a130","a131","a132","a133","a134","a135","a136","a137","a138","a139","a140","a141","a142","a143","a144","a145","a146","a147","a148","a149","a150","a151","a152","a153","a154","a155","a156","a157","a158","a159","a160","a161","a162","a163","a164","a165","a166","a167","a168","a169","a170","a171","a172","a173","a174","a175","a176","a177","a178","a179","a180","a181","a182","a183","a184","a185","a186","a187","a188","a189","a190","a191","a192","a193","a194","a195","a196","a197","a198","a199","a200","a201","a202","a203","a204","a205","a206","a207","a208","a209","a210","a211","a212","a213","a214","a215","a216","a217","a218","a219","a220","a221","a222","a223","a224","a225","a226","a227","a228","a229","a230","a231","a232","a233","a234","a235","a236","a237","a238","a239","a240","a241","a242","a243","a244","a245","a246","a247","a248","a249","a250","a251","a252","a253","a254","a255","s20","s21","s22","s23","s24","s25","s26","s27","s28","s29","s30","s31","s36","s37","s38","s39","s40","s41","s42","s43","s44","s45","s46","s47","s48","s49","s50","s51","s52","s53","s54","s55","s56","s57","s58","s59","s60","s61","s62","s63","s64","s65","s66","s67","s68","s69","s70","s71","s72","s73","s74","s75","s76","s77","s78","s79","s80","s81","s82","s83","s84","s85","s86","s87","s88","s89","s90","s91","s92","s93","s94","s95","s96","s97","s98","s99");
}

extern "C" void kernel_launch(void* const* d_in, const int* in_sizes, int n_in, void* d_out, int out_size, void* d_ws, size_t ws_size, hipStream_t stream) {
    (void)in_sizes; (void)n_in; (void)out_size; (void)ws_size;
    Args a;
    a.WreA = (const char*)d_in[1]; a.WimA = (const char*)d_in[2]; a.WreB = (const char*)d_in[6]; a.WimB = (const char*)d_in[7];
    a.ws = (char*)d_ws; a.out = (char*)d_out; a.x = (const char*)d_in[0];
    a.WdreA = (const char*)d_in[3]; a.WdimA = (const char*)d_in[4]; a.WdreB = (const char*)d_in[8]; a.WdimB = (const char*)d_in[9];
    fnet_kernel<<<256, 256, 0, stream>>>(a);
}
```
